```python
import torch

def _softmax(x, axis=-1):
    return torch.softmax(x, dim=axis)
def _gelu(x):
    return torch.nn.functional.gelu(x, approximate="tanh")
def _silu(x):
    return torch.nn.functional.silu(x)
def _sigmoid(x):
    return torch.sigmoid(x)
def _layernorm(x, axis=-1, eps=1e-5):
    m = x.mean(dim=axis, keepdim=True)
    v = x.var(dim=axis, keepdim=True, unbiased=False)
    return (x - m) / torch.sqrt(v + eps)
def _scatter_add(tgt, idx, src, axis=0):
    return tgt.clone().index_add_(axis, idx, src)
def _segment_sum(src, seg, n):
    out = torch.zeros((n,) + src.shape[1:], dtype=src.dtype)
    return out.index_add_(0, seg, src)
def _pad(x, pw):
    flat = tuple(v for pair in reversed(pw) for v in pair)
    return torch.nn.functional.pad(x, flat)

kernel_name = 'concat_heads__ssm_conv_h3__ssm_conv_s4__prompt'

def setup_inputs():

    _w = (lambda a: (lambda t: t.to(torch.bfloat16) if t.is_floating_point() else t)(torch.as_tensor(a)))
    def _A_setup():
        B, S, D = 1, 2048, 640
        ks = 4
        return {
            "q": _w((lambda *s: torch.randn(*s, dtype=torch.bfloat16) * 0.05)(B, S, D)),
            "k": _w((lambda *s: torch.randn(*s, dtype=torch.bfloat16) * 0.2)(B, S, D)),
            "v": _w((lambda *s: torch.randn(*s, dtype=torch.bfloat16) * 0.2)(B, S, D)),
            "shift_k": _w((lambda *s: torch.randn(*s, dtype=torch.bfloat16) * 0.2)(D, ks) * 0.5),
            "ssm_k": _w((lambda *s: torch.randn(*s, dtype=torch.bfloat16) * 0.8)(D, S) * (1.0 / S ** 0.5)),
            "ssm_D": _w((lambda *s: torch.randn(*s, dtype=torch.bfloat16) * 0.2)(D)),
        }
    _A_in = _A_setup()

    def _B_setup():
        B, L, D, N = 1, 2048, 640, 192
        A_re = -(lambda _x: torch.exp(torch.as_tensor(_x)))((lambda *s: torch.randn(*s, dtype=torch.bfloat16) * 3.2)(D, N))
        A_im = torch.pi * (lambda *s: torch.randn(*s, dtype=torch.bfloat16) * 0.05)(D, N)
        B_re = (lambda *s: torch.randn(*s, dtype=torch.bfloat16) * 0.1)(D, N)
        B_im = (lambda *s: torch.randn(*s, dtype=torch.bfloat16) * 0.1)(D, N)
        C_re = (lambda *s: torch.randn(*s, dtype=torch.bfloat16) * 0.1)(D, N)
        C_im = (lambda *s: torch.randn(*s, dtype=torch.bfloat16) * 0.1)(D, N)
        log_dt = (lambda *s: torch.randn(*s, dtype=torch.bfloat16) * 0.05)(D) * 0.5 - 4.0
        Dskip = (lambda *s: torch.randn(*s, dtype=torch.bfloat16) * 0.05)(D)
        return {
            "u": _w((lambda *s: torch.randn(*s, dtype=torch.bfloat16) * 0.05)(B, L, D)),
            "A_re": _w(A_re),
            "A_im": _w(A_im),
            "B_re": _w(B_re),
            "B_im": _w(B_im),
            "C_re": _w(C_re),
            "C_im": _w(C_im),
            "log_dt": _w(log_dt),
            "Dskip": _w(Dskip),
        }
    _B_in = _B_setup()

    return {
        "A_q": _A_in["q"],
        "A_k": _A_in["k"],
        "A_v": _A_in["v"],
        "A_shift_k": _A_in["shift_k"],
        "A_ssm_k": _A_in["ssm_k"],
        "A_ssm_D": _A_in["ssm_D"],
        "B_u": _B_in["u"],
        "B_A_re": _B_in["A_re"],
        "B_A_im": _B_in["A_im"],
        "B_B_re": _B_in["B_re"],
        "B_B_im": _B_in["B_im"],
        "B_C_re": _B_in["C_re"],
        "B_C_im": _B_in["C_im"],
        "B_log_dt": _B_in["log_dt"],
        "B_Dskip": _B_in["Dskip"],
    }

def reference(A_q, A_k, A_v, A_shift_k, A_ssm_k, A_ssm_D, B_u, B_A_re, B_A_im, B_B_re, B_B_im, B_C_re, B_C_im, B_log_dt, B_Dskip):
    def _A_ref(q, k, v, shift_k, ssm_k, ssm_D):
        B, S, D = q.shape
        ks = shift_k.shape[-1]
        k_pad = _pad(k, ((0, 0), (ks - 1, 0), (0, 0)))
        k_shift = torch.zeros_like(k)
        for t in range(ks):
            k_shift = k_shift + k_pad[:, t:t + S, :] * shift_k[:, ks - 1 - t][None, None, :]
        kv = k_shift * v
        L = 2 * S
        kv_pad = _pad(kv, ((0, 0), (0, S), (0, 0)))
        kf_pad = _pad(ssm_k, ((0, 0), (0, S)))
        kv_f = (lambda x, axes=(-2,-1): torch.fft.rfft2(x.float(), dim=axes))(kv_pad[:, None, :, :], axes=(1, 2))
        kf_f = (lambda x, axes=(-2,-1): torch.fft.rfft2(x.float(), dim=axes))(kf_pad[:, None, :], axes=(1, 2))
        kf_f = (lambda x, *d: x.permute(*d))(kf_f, 1, 2, 0)
        y_f = kv_f * kf_f[None, :, :, :]
        y = (lambda x, s, axes=(-2,-1): torch.fft.irfft2(x, s=s, dim=axes).to(torch.bfloat16))(y_f, (1, L), axes=(1, 2))[:, 0, :S, :]
        y = y + kv * ssm_D[None, None, :]
        return q * y

    def _B_ref(u, A_re, A_im, B_re, B_im, C_re, C_im, log_dt, Dskip):
        Bsz, L, D = u.shape
        dt = torch.exp(log_dt)[:, None]
        A = A_re + 1j * A_im
        Bc = B_re + 1j * B_im
        Cc = C_re + 1j * C_im
        logdA = dt * A
        dB = dt * Bc
        l = (lambda x, dt: x.to(dt))(torch.arange(L), A_re.dtype)
        P = torch.exp(logdA[:, :, None] * l[None, None, :])
        K = torch.einsum("dn,dnl,dn->dl", Cc, P, dB).real
        L2 = 2 * L
        Kp = _pad(K, ((0, 0), (0, L)))
        up = (lambda x, *d: x.permute(*d))(u, 0, 2, 1)
        up = _pad(up, ((0, 0), (0, 0), (0, L)))
        Kf = (lambda x, axes=(-2,-1): torch.fft.rfft2(x.float(), dim=axes))(Kp[:, None, :], axes=(-2, -1))
        Uf = (lambda x, axes=(-2,-1): torch.fft.rfft2(x.float(), dim=axes))(up[:, :, None, :], axes=(-2, -1))
        Yf = Uf * Kf[None, :, :, :]
        y = (lambda x, s, axes=(-2,-1): torch.fft.irfft2(x, s=s, dim=axes).to(torch.bfloat16))(Yf, (1, L2), axes=(-2, -1))[:, :, 0, :L]
        y = (lambda x, *d: x.permute(*d))(y, 0, 2, 1) + u * Dskip[None, None, :]
        return y

    a = _A_ref(A_q, A_k, A_v, A_shift_k, A_ssm_k, A_ssm_D)
    b = _B_ref(B_u, B_A_re, B_A_im, B_B_re, B_B_im, B_C_re, B_C_im, B_log_dt, B_Dskip)
    return torch.concatenate([a, b], axis=-1)
```

```cpp
#include <hip/hip_runtime.h>
#include <hip/hip_bf16.h>
#include <stdint.h>

#define S_ 2048
#define D_ 640
#define N_ 192

__device__ __forceinline__ float bf2f(unsigned short x) { return __uint_as_float(((unsigned)x) << 16); }
__device__ __forceinline__ unsigned short f2bf(float f) {
    unsigned u = __float_as_uint(f);
    u += 0x7FFF + ((u >> 16) & 1);
    return (unsigned short)(u >> 16);
}
__device__ __forceinline__ float rbf(float f) { return bf2f(f2bf(f)); }

__global__ void prep_x(const unsigned short* k, const unsigned short* v, const unsigned short* sk,
                       const unsigned short* u, float* xT) {
    int idx = blockIdx.x * blockDim.x + threadIdx.x;
    if (idx >= S_ * D_) return;
    int s = idx / D_, d = idx % D_;
    float acc = 0.f;
    for (int t = 0; t < 4; ++t) {
        int ss = s + t - 3;
        float kk = ss >= 0 ? bf2f(k[ss * D_ + d]) : 0.f;
        acc = rbf(acc + rbf(kk * bf2f(sk[d * 4 + 3 - t])));
    }
    xT[d * S_ + s] = rbf(acc * bf2f(v[idx]));
    xT[(D_ + d) * S_ + s] = bf2f(u[idx]);
}

__global__ void prep_k(const unsigned short* ssm_k, const unsigned short* Are, const unsigned short* Aim,
                       const unsigned short* Bre, const unsigned short* Bim, const unsigned short* Cre,
                       const unsigned short* Cim, const unsigned short* logdt, float* KT) {
    int idx = blockIdx.x * blockDim.x + threadIdx.x;
    if (idx >= S_ * D_) return;
    int d = idx / S_, l = idx % S_;
    KT[idx] = bf2f(ssm_k[idx]);
    float dt = rbf(expf(bf2f(logdt[d])));
    float lf = rbf((float)l);
    float acc = 0.f;
    for (int n = 0; n < N_; ++n) {
        int o = d * N_ + n;
        float zr = dt * bf2f(Are[o]), zi = dt * bf2f(Aim[o]);
        float br = dt * bf2f(Bre[o]), bi = dt * bf2f(Bim[o]);
        float cr = bf2f(Cre[o]), ci = bf2f(Cim[o]);
        float m = expf(zr * lf);
        float sn, cs;
        sincosf(zi * lf, &sn, &cs);
        float pr = m * cs, pi = m * sn;
        float wr = cr * br - ci * bi, wi = cr * bi + ci * br;
        acc += wr * pr - wi * pi;
    }
    KT[D_ * S_ + idx] = acc;
}

__global__ void conv(const float* xT, const float* KT, const unsigned short* q, const unsigned short* ssmD,
                     const unsigned short* Dskip, unsigned short* out) {
    __shared__ float xs[S_], ks[S_];
    int c = blockIdx.x;
    for (int i = threadIdx.x; i < S_; i += blockDim.x) { xs[i] = xT[c * S_ + i]; ks[i] = KT[c * S_ + i]; }
    __syncthreads();
    for (int i = threadIdx.x; i < S_; i += blockDim.x) {
        float acc = 0.f;
        for (int j = 0; j <= i; ++j) acc += ks[i - j] * xs[j];
        float y = rbf(acc);
        if (c < D_) {
            y = rbf(y + rbf(xs[i] * bf2f(ssmD[c])));
            y = bf2f(q[i * D_ + c]) * y;
        } else {
            y = y + rbf(xs[i] * bf2f(Dskip[c - D_]));
        }
        out[i * (2 * D_) + c] = f2bf(y);
    }
}

extern "C" void kernel_launch(void* const* d_in, const int* in_sizes, int n_in,
                              void* d_out, int out_size, void* d_ws, size_t ws_size,
                              hipStream_t stream) {
    typedef const unsigned short* P;
    float* xT = (float*)d_ws;
    float* KT = xT + 2 * D_ * S_;
    prep_x<<<(S_ * D_ + 255) / 256, 256, 0, stream>>>((P)d_in[1], (P)d_in[2], (P)d_in[3], (P)d_in[6], xT);
    prep_k<<<(S_ * D_ + 255) / 256, 256, 0, stream>>>((P)d_in[4], (P)d_in[7], (P)d_in[8], (P)d_in[9], (P)d_in[10],
                                                     (P)d_in[11], (P)d_in[12], (P)d_in[13], KT);
    conv<<<2 * D_, 256, 0, stream>>>(xT, KT, (P)d_in[0], (P)d_in[5], (P)d_in[14], (unsigned short*)d_out);
}
```

```cpp
#include <hip/hip_runtime.h>
#include <hip/hip_bf16.h>
#include <stdint.h>

#define S_ 2048
#define D_ 640
#define N_ 192

__device__ __forceinline__ float bf2f(unsigned short x) { return __uint_as_float(((unsigned)x) << 16); }
__device__ __forceinline__ unsigned short f2bf(float f) {
    unsigned u = __float_as_uint(f);
    u += 0x7FFF + ((u >> 16) & 1);
    return (unsigned short)(u >> 16);
}
__device__ __forceinline__ float rbf(float f) { return bf2f(f2bf(f)); }

__global__ void prep_x(const unsigned short* k, const unsigned short* v, const unsigned short* sk,
                       const unsigned short* u, float* xT) {
    int idx = blockIdx.x * blockDim.x + threadIdx.x;
    if (idx >= S_ * D_) return;
    int s = idx / D_, d = idx % D_;
    float acc = 0.f;
    for (int t = 0; t < 4; ++t) {
        int ss = s + t - 3;
        float kk = ss >= 0 ? bf2f(k[ss * D_ + d]) : 0.f;
        acc = rbf(acc + rbf(kk * bf2f(sk[d * 4 + 3 - t])));
    }
    xT[d * S_ + s] = rbf(acc * bf2f(v[idx]));
    xT[(D_ + d) * S_ + s] = bf2f(u[idx]);
}

__global__ void prep_k(const unsigned short* ssm_k, const unsigned short* Are, const unsigned short* Aim,
                       const unsigned short* Bre, const unsigned short* Bim, const unsigned short* Cre,
                       const unsigned short* Cim, const unsigned short* logdt, float* KT) {
    int idx = blockIdx.x * blockDim.x + threadIdx.x;
    if (idx >= S_ * D_) return;
    int d = idx / S_, l = idx % S_;
    KT[idx] = bf2f(ssm_k[idx]);
    float dt = rbf(expf(bf2f(logdt[d])));
    float lf = rbf((float)l);
    float acc = 0.f;
    for (int n = 0; n < N_; ++n) {
        int o = d * N_ + n;
        float zr = dt * bf2f(Are[o]), zi = dt * bf2f(Aim[o]);
        float br = dt * bf2f(Bre[o]), bi = dt * bf2f(Bim[o]);
        float cr = bf2f(Cre[o]), ci = bf2f(Cim[o]);
        float m = expf(zr * lf);
        float sn, cs;
        sincosf(zi * lf, &sn, &cs);
        float pr = m * cs, pi = m * sn;
        float wr = cr * br - ci * bi, wi = cr * bi + ci * br;
        acc += wr * pr - wi * pi;
    }
    KT[D_ * S_ + idx] = acc;
}

__global__ void conv(const float* xT, const float* KT, const unsigned short* q, const unsigned short* ssmD,
                     const unsigned short* Dskip, unsigned short* out) {
    __shared__ float xs[S_], ks[S_];
    int c = blockIdx.x;
    for (int i = threadIdx.x; i < S_; i += blockDim.x) { xs[i] = xT[c * S_ + i]; ks[i] = KT[c * S_ + i]; }
    __syncthreads();
    for (int i = threadIdx.x; i < S_; i += blockDim.x) {
        float acc = 0.f;
        for (int j = 0; j <= i; ++j) acc += ks[i - j] * xs[j];
        float y = rbf(acc);
        if (c < D_) {
            y = rbf(y + rbf(xs[i] * bf2f(ssmD[c])));
            y = bf2f(q[i * D_ + c]) * y;
        } else {
            y = y + rbf(xs[i] * bf2f(Dskip[c - D_]));
        }
        out[i * (2 * D_) + c] = f2bf(y);
    }
}


typedef unsigned int u32x4 __attribute__((ext_vector_type(4)));
struct Ptrs { const char* p[15]; };
__global__ __launch_bounds__(512) void floork(Ptrs P, unsigned* sink, unsigned long long* stamps) {
    unsigned long long t0 = __builtin_amdgcn_s_memrealtime();
    int b = blockIdx.x, tid = threadIdx.x;
    int gid = b * 512 + tid;
    u32x4 acc = {0,0,0,0};
    u32x4 v[11];
    const int big[5] = {0,1,2,6,4};
#pragma unroll
    for (int i = 0; i < 5; ++i) v[i] = ((const u32x4*)P.p[big[i]])[gid];
#pragma unroll
    for (int i = 0; i < 5; ++i) v[5+i] = (gid < 163840 - 131072) ? ((const u32x4*)P.p[big[i]])[gid + 131072] : acc;
    int pi = gid / 15360, po = gid % 15360;
    v[10] = pi < 6 ? ((const u32x4*)P.p[7 + (pi < 6 ? pi : 0)])[po] : acc;
#pragma unroll
    for (int i = 0; i < 11; ++i) acc ^= v[i];
    unsigned x = acc.x ^ acc.y ^ acc.z ^ acc.w;
    if (x == 0x12345678) sink[tid] = x;
    asm volatile("s_waitcnt vmcnt(0)" ::: "memory");
    unsigned long long t2 = __builtin_amdgcn_s_memrealtime();
    if ((tid & 63) == 0) { int w = tid >> 6; stamps[(b * 8 + w) * 2] = t0; stamps[(b * 8 + w) * 2 + 1] = t2; }
}
__global__ void delayk(const unsigned long long* stamps) {
    __shared__ unsigned long long smn[256], smx[256];
    unsigned long long mn = ~0ull, mx = 0;
    for (int i = threadIdx.x; i < 2048; i += 256) { mn = stamps[2*i] < mn ? stamps[2*i] : mn; mx = stamps[2*i+1] > mx ? stamps[2*i+1] : mx; }
    smn[threadIdx.x] = mn; smx[threadIdx.x] = mx;
    __syncthreads();
    for (int o = 128; o > 0; o >>= 1) { if (threadIdx.x < o) { if (smn[threadIdx.x + o] < smn[threadIdx.x]) smn[threadIdx.x] = smn[threadIdx.x + o]; if (smx[threadIdx.x + o] > smx[threadIdx.x]) smx[threadIdx.x] = smx[threadIdx.x + o]; } __syncthreads(); }
    unsigned long long span = smx[0] - smn[0];
    if (span > 5000) span = 5000;
    unsigned long long t0 = __builtin_amdgcn_s_memrealtime();
    while (__builtin_amdgcn_s_memrealtime() - t0 < span * 100) { __builtin_amdgcn_s_sleep(10); }
}

extern "C" void kernel_launch(void* const* d_in, const int* in_sizes, int n_in,
                              void* d_out, int out_size, void* d_ws, size_t ws_size,
                              hipStream_t stream) {
    typedef const unsigned short* P;
    float* xT = (float*)d_ws;
    float* KT = xT + 2 * D_ * S_;
    Ptrs PP; for (int i = 0; i < 15; ++i) PP.p[i] = (const char*)d_in[i];
    unsigned long long* stamps = (unsigned long long*)((char*)d_ws + (128 << 20));
    floork<<<256, 512, 0, stream>>>(PP, (unsigned*)((char*)d_ws + (129 << 20)), stamps);
    delayk<<<1, 256, 0, stream>>>(stamps);
    prep_x<<<(S_ * D_ + 255) / 256, 256, 0, stream>>>((P)d_in[1], (P)d_in[2], (P)d_in[3], (P)d_in[6], xT);
    prep_k<<<(S_ * D_ + 255) / 256, 256, 0, stream>>>((P)d_in[4], (P)d_in[7], (P)d_in[8], (P)d_in[9], (P)d_in[10],
                                                     (P)d_in[11], (P)d_in[12], (P)d_in[13], KT);
    conv<<<2 * D_, 256, 0, stream>>>(xT, KT, (P)d_in[0], (P)d_in[5], (P)d_in[14], (unsigned short*)d_out);
}
```

```cpp
#include <hip/hip_runtime.h>
#include <hip/hip_bf16.h>
#include <stdint.h>

typedef unsigned int u32;
typedef unsigned short u16;
typedef u32 u32x2 __attribute__((ext_vector_type(2)));
typedef u32 u32x4 __attribute__((ext_vector_type(4)));
typedef float f32x4 __attribute__((ext_vector_type(4)));
typedef float f32x16 __attribute__((ext_vector_type(16)));
typedef __bf16 bf16x8 __attribute__((ext_vector_type(8)));
typedef __bf16 bf16x2 __attribute__((ext_vector_type(2)));
typedef float f32x2 __attribute__((ext_vector_type(2)));
#define LDS __attribute__((address_space(3)))
#define MEMBAR() asm volatile("" ::: "memory")

constexpr int S_ = 2048, D_ = 640, N_ = 192;
constexpr int ROWB = D_ * 2;
constexpr int OROWB = 2 * D_ * 2;

constexpr int KC0 = 8;
constexpr int KC1 = KC0 + 4224;
constexpr int XR0 = 8384;
constexpr int XR1 = XR0 + 4640;
constexpr int CHB = XR1 + 4544;
constexpr int STG = 8 * CHB;
constexpr int LDS_BYTES = STG + 8 * 2048;
constexpr int SCR = 4 * CHB;
constexpr int SCRB = 9216;
constexpr int BSTG = SCR;
constexpr int BPART = SCR + 8192;

struct Params {
    const char* q; const char* k; const char* v; const char* shiftk; const char* ssmk; const char* ssmD;
    const char* u; const char* Are; const char* Aim; const char* Bre; const char* Bim; const char* Cre;
    const char* Cim; const char* logdt; const char* Dskip; char* out;
};

__device__ __forceinline__ float bflo(u32 x) { return __uint_as_float(x << 16); }
__device__ __forceinline__ float bfhi(u32 x) { return __uint_as_float(x & 0xffff0000u); }
__device__ __forceinline__ u32 pk(float lo, float hi) {
    f32x2 f = {lo, hi};
    bf16x2 b = __builtin_convertvector(f, bf16x2);
    return __builtin_bit_cast(u32, b);
}
__device__ __forceinline__ u32 shr1(u32 old, u32 src) {
    return __builtin_amdgcn_update_dpp(old, src, 0x138, 0xf, 0xf, false);
}
__device__ __forceinline__ bf16x8 rdA(u32 addr, int off) {
    u32x2 lo = *(const LDS u32x2*)(addr + off);
    MEMBAR();
    u32x2 hi = *(const LDS u32x2*)(addr + off + 8);
    MEMBAR();
    u32x4 v = {lo.x, lo.y, hi.x, hi.y};
    return __builtin_bit_cast(bf16x8, v);
}
__device__ __forceinline__ bf16x8 rdB(u32 addr, int off) {
    u32x4 v = *(const LDS u32x4*)(addr + off);
    MEMBAR();
    return __builtin_bit_cast(bf16x8, v);
}

template <int S0, int T0>
__device__ __forceinline__ void conv_stage(f32x4 (&acc)[8], u32 aA, u32 aB) {
    bf16x8 a[2][4], b[2][4];
#pragma unroll
    for (int i = 0; i < 4; ++i) { a[0][i] = rdA(aA, 512 * (S0 + i)); b[0][i] = rdB(aB, 512 * (7 - T0 - i)); }
#pragma unroll
    for (int rho = 0; rho < 8; ++rho) {
        if (rho < 7) {
#pragma unroll
            for (int i = 0; i < 4; ++i) {
                a[(rho + 1) & 1][i] = rdA(aA, 512 * (S0 + i) + 64 * (rho + 1));
                b[(rho + 1) & 1][i] = rdB(aB, 512 * (7 - T0 - i) + 64 * (rho + 1));
            }
        }
        __builtin_amdgcn_sched_barrier(0);
#pragma unroll
        for (int s = 0; s < 4; ++s)
#pragma unroll
            for (int t = 0; t < 4; ++t)
                if (S0 + s + T0 + t <= 7)
                    acc[S0 + s + T0 + t] = __builtin_amdgcn_mfma_f32_16x16x32_bf16(a[rho & 1][s], b[rho & 1][t], acc[S0 + s + T0 + t], 0, 0, 0);
        __builtin_amdgcn_sched_barrier(0);
    }
}

template <int T0>
__device__ __forceinline__ void stage4(const f32x4 (&acc)[8], u32 base, int lane) {
    int c = lane & 15, g = lane >> 4;
    u32 a = base + 2 * (32 * (c >> 1) + 8 * g + 2 * (c & 1));
#pragma unroll
    for (int t = 0; t < 4; ++t) {
        *(LDS u32*)(a + 512 * t) = pk(acc[T0 + t][0], acc[T0 + t][1]);
        *(LDS u32*)(a + 512 * t + 8) = pk(acc[T0 + t][2], acc[T0 + t][3]);
    }
}

struct KV { u32x4 h[3]; u32x4 k[4]; u32x4 v[4]; };

__device__ __forceinline__ void load_kv(KV& r, const Params& P, int ch0, int blk, int lane) {
    const char* kb = P.k + ch0 * 2;
    const char* vb = P.v + ch0 * 2;
    int row0 = 256 * blk;
#pragma unroll
    for (int e = 0; e < 3; ++e) {
        int hr = row0 - 3 + e;
        r.h[e] = *(const u32x4*)(kb + (hr < 0 ? 0 : hr) * ROWB);
    }
    u32 off = (u32)(row0 + 4 * lane) * ROWB;
#pragma unroll
    for (int e = 0; e < 4; ++e) r.k[e] = *(const u32x4*)(kb + off + e * ROWB);
#pragma unroll
    for (int e = 0; e < 4; ++e) r.v[e] = *(const u32x4*)(vb + off + e * ROWB);
}

__device__ __forceinline__ void make_x(const KV& r, const float (&sw)[8][4], u32 lds0, int blk, int lane) {
    int L = 64 * blk + lane;
    u32 a1 = lds0 + XR1 + 4088 - 8 * L;
    u32 a0 = lds0 + XR0 + 4084 - 8 * L;
#pragma unroll
    for (int d = 0; d < 4; ++d) {
        u32 kd[7];
#pragma unroll
        for (int e = 0; e < 3; ++e) kd[e] = shr1(blk == 0 ? 0u : r.h[e][d], r.k[e + 1][d]);
#pragma unroll
        for (int e = 0; e < 4; ++e) kd[3 + e] = r.k[e][d];
#pragma unroll
        for (int hl = 0; hl < 2; ++hl) {
            int ch = 2 * d + hl;
            float kf[7], x[4];
#pragma unroll
            for (int e = 0; e < 7; ++e) kf[e] = hl ? bfhi(kd[e]) : bflo(kd[e]);
#pragma unroll
            for (int e = 0; e < 4; ++e) {
                float s = kf[e] * sw[ch][3];
                s = fmaf(kf[e + 1], sw[ch][2], s);
                s = fmaf(kf[e + 2], sw[ch][1], s);
                s = fmaf(kf[e + 3], sw[ch][0], s);
                x[e] = s * (hl ? bfhi(r.v[e][d]) : bflo(r.v[e][d]));
            }
            u32 d0 = pk(x[3], x[2]), d1 = pk(x[1], x[0]);
            *(LDS u32x2*)(a1 + ch * CHB) = u32x2{d0, d1};
            *(LDS u32*)(a0 + ch * CHB) = d0;
            *(LDS u32*)(a0 + ch * CHB + 4) = d1;
        }
    }
}

__device__ __forceinline__ void zero_pads(u32 lds0, int nch, int tid) {
    if (tid < 253) {
        u32 off;
        if (tid < 14) off = KC0 + 4 * tid;
        else if (tid < 28) off = KC1 + 4 * (tid - 14);
        else if (tid < 141) off = XR0 + 4092 + 4 * (tid - 28);
        else off = XR1 + 4096 + 4 * (tid - 141);
        for (int c = 0; c < nch; ++c) *(LDS u32*)(lds0 + c * CHB + off) = 0u;
    }
}

__device__ __forceinline__ void cexp(float zr2, float zrev, float p, float& re, float& im) {
    float m = __builtin_amdgcn_exp2f(zr2 * p);
    float r = __builtin_amdgcn_fractf(zrev * p);
    re = m * __builtin_amdgcn_cosf(r);
    im = m * __builtin_amdgcn_sinf(r);
}
#define CMUL(or_, oi_, ar, ai, br, bi) { float tr_ = ar * br - ai * bi; float ti_ = ar * bi + ai * br; or_ = tr_; oi_ = ti_; }

__global__ __launch_bounds__(512, 2) void fused_kernel(Params P) {
    __shared__ __attribute__((aligned(16))) char smem[LDS_BYTES];
    const u32 lds0 = (u32)(size_t)(LDS char*)smem;
    const int tid = threadIdx.x, lane = tid & 63;
    const int wave = __builtin_amdgcn_readfirstlane(tid >> 6);
    const int g8 = blockIdx.x & 7, j = blockIdx.x >> 3;

    const int fr = lane & 15, fg = lane >> 4;
    const u32 relA = ((fr & 1) ? KC1 : KC0) + 8 * (fr >> 1) + 16 * fg;
    const u32 relB = ((fr & 1) ? XR1 : XR0) + 4032 - 3584 - 64 * (fr >> 1) + 16 * fg;

    if (j < 10) {
        const int ch0 = 80 * g8 + 8 * j;
        KV r;
        u32x4 kc[2][4];
        if (wave < 4) {
            load_kv(r, P, ch0, wave, lane);
        } else {
#pragma unroll
            for (int c = 0; c < 2; ++c)
#pragma unroll
                for (int i = 0; i < 4; ++i)
                    kc[c][i] = *(const u32x4*)(P.ssmk + (size_t)(ch0 + 2 * (wave - 4) + c) * (S_ * 2) + (i * 64 + lane) * 16);
        }
        float sw[8][4];
        {
            const u32* sp = (const u32*)(P.shiftk + ch0 * 8);
#pragma unroll
            for (int c = 0; c < 8; ++c) {
                u32 a = sp[2 * c], b = sp[2 * c + 1];
                sw[c][0] = bflo(a); sw[c][1] = bfhi(a); sw[c][2] = bflo(b); sw[c][3] = bfhi(b);
            }
        }
        if (wave < 4) {
            make_x(r, sw, lds0, wave, lane);
        } else {
            zero_pads(lds0, 8, tid - 256);
#pragma unroll
            for (int c = 0; c < 2; ++c) {
                int ch = 2 * (wave - 4) + c;
                float dsk = bflo(*(const u16*)(P.ssmD + (ch0 + ch) * 2));
                if (lane == 0) {
                    u32 d0 = kc[c][0].x;
                    kc[c][0].x = (pk(bflo(d0) + dsk, 0.f) & 0xffffu) | (d0 & 0xffff0000u);
                }
                u32 prev = 0;
#pragma unroll
                for (int i = 0; i < 4; ++i) {
                    u32x4 w = kc[c][i];
                    u32 pm = shr1(prev, w.w);
                    prev = __builtin_amdgcn_readlane(w.w, 63);
                    u32x4 s;
                    s.x = __builtin_amdgcn_alignbit(w.x, pm, 16);
                    s.y = __builtin_amdgcn_alignbit(w.y, w.x, 16);
                    s.z = __builtin_amdgcn_alignbit(w.z, w.y, 16);
                    s.w = __builtin_amdgcn_alignbit(w.w, w.z, 16);
                    u32 a = lds0 + ch * CHB + 56 + (i * 64 + lane) * 16;
                    *(LDS u32x4*)(a + KC1) = w;
                    *(LDS u32x4*)(a + KC0) = s;
                }
            }
            load_kv(r, P, ch0, wave, lane);
        }
        u32x4 q1[4], q2[2];
        __syncthreads();
        if (wave < 4) {
#pragma unroll
            for (int e = 0; e < 4; ++e) q1[e] = *(const u32x4*)(P.q + ch0 * 2 + (u32)(4 * tid + e) * ROWB);
        }
        f32x4 acc[8];
#pragma unroll
        for (int t = 0; t < 8; ++t) acc[t] = f32x4{0.f, 0.f, 0.f, 0.f};
        u32 aA = lds0 + wave * CHB + relA, aB = lds0 + wave * CHB + relB;
        asm volatile("" : "+v"(aA), "+v"(aB));
        conv_stage<0, 0>(acc, aA, aB);
        stage4<0>(acc, lds0 + STG + wave * 2048, lane);
        if (wave >= 4) make_x(r, sw, lds0, wave, lane);
#pragma unroll
        for (int e = 0; e < 2; ++e) q2[e] = *(const u32x4*)(P.q + ch0 * 2 + (u32)(1024 + 2 * tid + e) * ROWB);
        __syncthreads();
        if (wave < 4) {
            u32x2 y[8];
#pragma unroll
            for (int c = 0; c < 8; ++c) y[c] = *(const LDS u32x2*)(lds0 + STG + c * 2048 + 8 * tid);
#pragma unroll
            for (int e = 0; e < 4; ++e) {
                u32x4 o;
#pragma unroll
                for (int d = 0; d < 4; ++d) {
                    u32 ya = y[2 * d][e >> 1], yb = y[2 * d + 1][e >> 1];
                    float fa = (e & 1) ? bfhi(ya) : bflo(ya), fb = (e & 1) ? bfhi(yb) : bflo(yb);
                    o[d] = pk(fa * bflo(q1[e][d]), fb * bfhi(q1[e][d]));
                }
                *(u32x4*)(P.out + ch0 * 2 + (u32)(4 * tid + e) * OROWB) = o;
            }
        }
        conv_stage<4, 0>(acc, aA, aB);
        conv_stage<0, 4>(acc, aA, aB);
        stage4<4>(acc, lds0 + wave * CHB, lane);
        __syncthreads();
        {
            u32 y[8];
#pragma unroll
            for (int c = 0; c < 8; ++c) y[c] = *(const LDS u32*)(lds0 + c * CHB + 4 * tid);
#pragma unroll
            for (int e = 0; e < 2; ++e) {
                u32x4 o;
#pragma unroll
                for (int d = 0; d < 4; ++d) {
                    float fa = e ? bfhi(y[2 * d]) : bflo(y[2 * d]), fb = e ? bfhi(y[2 * d + 1]) : bflo(y[2 * d + 1]);
                    o[d] = pk(fa * bflo(q2[e][d]), fb * bfhi(q2[e][d]));
                }
                *(u32x4*)(P.out + ch0 * 2 + (u32)(1024 + 2 * tid + e) * OROWB) = o;
            }
        }
        return;
    }

    const bool isB = j < 30;
    const int ch0 = 80 * g8 + 4 * (j - 10);
    const int chw = wave & 3, part = wave >> 2;
    u16 pr[3][6];
    float ldt = 0.f;
    if (isB) {
        int o = ((ch0 + chw) * N_ + 96 * part + (lane & 31)) * 2;
#pragma unroll
        for (int t = 0; t < 3; ++t) {
            pr[t][0] = *(const u16*)(P.Are + o + 64 * t); pr[t][1] = *(const u16*)(P.Aim + o + 64 * t);
            pr[t][2] = *(const u16*)(P.Bre + o + 64 * t); pr[t][3] = *(const u16*)(P.Bim + o + 64 * t);
            pr[t][4] = *(const u16*)(P.Cre + o + 64 * t); pr[t][5] = *(const u16*)(P.Cim + o + 64 * t);
        }
        ldt = bflo(*(const u16*)(P.logdt + (ch0 + chw) * 2));
    }
    u32 pf[3];
    {
        int pw = (j - 10) * 8 + wave;
        int lr = lane / 3, ls = lane - 3 * lr;
        int sec0 = (160 * g8) >> 6;
#pragma unroll
        for (int i = 0; i < 3; ++i) {
            int it = pw + 176 * i;
            const char* p;
            if (it >= 98 && it < 178) {
                p = P.ssmk + (size_t)(80 * g8 + (it - 98)) * (S_ * 2) + lane * 64;
            } else {
                const char* base; int rb;
                if (it < 98) { base = (it & 1) ? P.v : P.k; rb = it >> 1; }
                else if (it < 276) { base = P.u; rb = it - 178; }
                else if (it < 374) { base = (it & 1) ? P.v : P.k; rb = 49 + ((it - 276) >> 1); }
                else { base = P.q; rb = (it - 374) < 98 ? (it - 374) : 97; }
                int row = rb * 21 + lr;
                row = row > 2047 ? 2047 : row;
                p = base + row * ROWB + (sec0 + ls) * 64;
            }
            pf[i] = *(const u32*)p;
        }
    }
    if (!isB) {
        asm volatile("" :: "v"(pf[0]), "v"(pf[1]), "v"(pf[2]));
        return;
    }
    u32x2 ur[4];
#pragma unroll
    for (int e = 0; e < 4; ++e) ur[e] = *(const u32x2*)(P.u + ch0 * 2 + (u32)(4 * tid + e) * ROWB);
    if (tid < 256) zero_pads(lds0, 4, tid);

    const u32 scr = lds0 + SCR + wave * SCRB;
    {
        float dt = bflo(pk(expf(ldt), 0.f));
        const bool rows = lane >= 32;
        const float p1 = rows ? 1.f : 8.f, p2 = rows ? 256.f : 64.f;
        const float sg = rows ? -1.f : 1.f;
        f32x16 kacc;
#pragma unroll
        for (int i = 0; i < 16; ++i) kacc[i] = 0.f;
        const u32 wa = scr + (rows ? 4608 : 0) + 4 * (lane & 31);
        const u32 ra = scr + 144 * (lane & 31) + 16 * (lane >> 5);
#pragma unroll 1
        for (int t = 0; t < 3; ++t) {
            u16 c0 = t == 0 ? pr[0][0] : (t == 1 ? pr[1][0] : pr[2][0]);
            u16 c1 = t == 0 ? pr[0][1] : (t == 1 ? pr[1][1] : pr[2][1]);
            u16 c2 = t == 0 ? pr[0][2] : (t == 1 ? pr[1][2] : pr[2][2]);
            u16 c3 = t == 0 ? pr[0][3] : (t == 1 ? pr[1][3] : pr[2][3]);
            u16 c4 = t == 0 ? pr[0][4] : (t == 1 ? pr[1][4] : pr[2][4]);
            u16 c5 = t == 0 ? pr[0][5] : (t == 1 ? pr[1][5] : pr[2][5]);
            float zr2 = dt * bflo(c0) * 1.4426950408889634f;
            float zrev = dt * bflo(c1) * 0.15915494309189535f;
            float br = dt * bflo(c2), bi = dt * bflo(c3), cr = bflo(c4), ci = bflo(c5);
            float wr = rows ? (cr * br - ci * bi) : 1.f, wi = rows ? (cr * bi + ci * br) : 0.f;
            float P1r, P1i, P2r, P2i, P3r, P3i, P4r, P4i, P5r, P5i;
            cexp(zr2, zrev, p1, P1r, P1i);
            cexp(zr2, zrev, p2, P2r, P2i);
            P3r = P2r * P2r - P2i * P2i; P3i = 2.f * P2r * P2i;
            P4r = P1r * P1r - P1i * P1i; P4i = 2.f * P1r * P1i;
            P5r = P4r * P4r - P4i * P4i; P5i = 2.f * P4r * P4i;
            float sr[4], si[4], gr[4], gi[4];
            sr[0] = wr; si[0] = wi;
            CMUL(sr[1], si[1], wr, wi, P2r, P2i);
            float tr, ti;
            CMUL(tr, ti, wr, wi, P3r, P3i);
            float xr = rows ? P5r : P2r, xi = rows ? P5i : P2i;
            float ur_, ui_;
            CMUL(ur_, ui_, tr, ti, xr, xi);
            sr[2] = rows ? ur_ : tr; si[2] = rows ? ui_ : ti;
            sr[3] = rows ? tr : ur_; si[3] = rows ? ti : ui_;
            gr[0] = P1r; gi[0] = P1i;
            gr[1] = rows ? P4r : P1r; gi[1] = rows ? P4i : P1i;
            gr[2] = rows ? P2r : P1r; gi[2] = rows ? P2i : P1i;
            gr[3] = gr[2]; gi[3] = gi[2];
#pragma unroll
            for (int q = 0; q < 4; ++q) { si[q] *= sg; gi[q] *= sg; }
#pragma unroll
            for (int k = 0; k < 8; ++k) {
#pragma unroll
                for (int q = 0; q < 4; ++q) {
                    *(LDS u32*)(wa + 144 * (8 * q + k)) = pk(sr[q], si[q]);
                    if (k < 7) CMUL(sr[q], si[q], sr[q], si[q], gr[q], gi[q]);
                }
            }
#pragma unroll
            for (int ks = 0; ks < 4; ++ks) {
                u32x4 av = *(const LDS u32x4*)(ra + 4608 + 32 * ks);
                u32x4 bv = *(const LDS u32x4*)(ra + 32 * ks);
                kacc = __builtin_amdgcn_mfma_f32_32x32x16_bf16(__builtin_bit_cast(bf16x8, av), __builtin_bit_cast(bf16x8, bv), kacc, 0, 0, 0);
            }
            MEMBAR();
        }
        const int col = lane & 31, h = lane >> 5;
        const u32 tb = scr;
#pragma unroll
        for (int i = 0; i < 4; ++i) *(LDS float*)(tb + 4 * (8 * col + 4 * h + i)) = kacc[i];
        if (h == 0) {
#pragma unroll
            for (int i = 0; i < 4; ++i) *(LDS float*)(tb + 4 * (256 + 4 * col + i)) = kacc[4 + i];
            *(LDS float*)(tb + 4 * (384 + 2 * col + 1)) = kacc[8];
            *(LDS float*)(tb + 4 * (448 + 2 * col + 1)) = kacc[9];
            *(LDS float*)(tb + 4 * (384 + 2 * col)) = kacc[12];
            *(LDS float*)(tb + 4 * (448 + 2 * col)) = kacc[13];
            *(LDS float*)(tb + 4 * (512 + col)) = kacc[14];
            *(LDS float*)(tb + 4 * (544 + col)) = kacc[15];
        } else {
            *(LDS float*)(tb + 4 * (576 + col)) = kacc[12];
            *(LDS float*)(tb + 4 * (608 + col)) = kacc[13];
            if (col == 0) *(LDS float*)(tb + 4 * 640) = kacc[14];
        }
    }
    {
        asm volatile("" :: "v"(pf[0]), "v"(pf[1]), "v"(pf[2]));
        u32 a1 = lds0 + XR1 + 4088 - 8 * tid;
        u32 a0 = lds0 + XR0 + 4084 - 8 * tid;
#pragma unroll
        for (int c = 0; c < 4; ++c) {
            u32 sel = (c & 1) ? 0x03020706u : 0x01000504u;
            u32 d0 = __builtin_amdgcn_perm(ur[3][c >> 1], ur[2][c >> 1], sel);
            u32 d1 = __builtin_amdgcn_perm(ur[1][c >> 1], ur[0][c >> 1], sel);
            *(LDS u32x2*)(a1 + c * CHB) = u32x2{d0, d1};
            *(LDS u32*)(a0 + c * CHB) = d0;
            *(LDS u32*)(a0 + c * CHB + 4) = d1;
        }
    }
    __syncthreads();
    {
        const u32 ta = lds0 + SCR + chw * SCRB, tbb = lds0 + SCR + (chw + 4) * SCRB;
        float dsk = bflo(*(const u16*)(P.Dskip + (ch0 + chw) * 2));
#pragma unroll
        for (int it = 0; it < 2; ++it) {
            int n = 128 * part + 64 * it + lane;
            float kv[9];
#pragma unroll
            for (int e = 0; e < 9; ++e) {
                int l = 8 * n - 1 + e;
                int lc = l < 0 ? 0 : l;
                int bits = (int)(pk((float)lc, 0.f) & 0xffffu);
                int I = lc < 128 ? lc : bits - (134 << 7) + 128;
                float val = *(const LDS float*)(ta + 4 * I) + *(const LDS float*)(tbb + 4 * I);
                if (l == 0) val += dsk;
                kv[e] = l < 0 ? 0.f : val;
            }
            u32x4 w1 = {pk(kv[1], kv[2]), pk(kv[3], kv[4]), pk(kv[5], kv[6]), pk(kv[7], kv[8])};
            u32x4 w0 = {pk(kv[0], kv[1]), pk(kv[2], kv[3]), pk(kv[4], kv[5]), pk(kv[6], kv[7])};
            u32 a = lds0 + chw * CHB + 56 + n * 16;
            *(LDS u32x4*)(a + KC1) = w1;
            *(LDS u32x4*)(a + KC0) = w0;
        }
    }
    __syncthreads();
    f32x4 acc[8];
#pragma unroll
    for (int t = 0; t < 8; ++t) acc[t] = f32x4{0.f, 0.f, 0.f, 0.f};
    u32 aA = lds0 + chw * CHB + relA, aB = lds0 + chw * CHB + relB;
    asm volatile("" : "+v"(aA), "+v"(aB));
    if (part == 0) {
        conv_stage<0, 0>(acc, aA, aB);
        stage4<0>(acc, lds0 + BSTG + chw * 2048, lane);
#pragma unroll
        for (int t = 0; t < 3; ++t) *(LDS f32x4*)(lds0 + BPART + chw * 3072 + t * 1024 + lane * 16) = acc[4 + t];
    } else {
        conv_stage<4, 0>(acc, aA, aB);
    }
    __syncthreads();
    char* ob = P.out + D_ * 2 + ch0 * 2;
    if (part == 0) {
        u32x2 y[4];
#pragma unroll
        for (int c = 0; c < 4; ++c) y[c] = *(const LDS u32x2*)(lds0 + BSTG + c * 2048 + 8 * tid);
#pragma unroll
        for (int e = 0; e < 4; ++e) {
            u32 sel = (e & 1) ? 0x07060302u : 0x05040100u;
            u32x2 o = {__builtin_amdgcn_perm(y[1][e >> 1], y[0][e >> 1], sel), __builtin_amdgcn_perm(y[3][e >> 1], y[2][e >> 1], sel)};
            *(u32x2*)(ob + (u32)(4 * tid + e) * OROWB) = o;
        }
    } else {
#pragma unroll
        for (int t = 0; t < 3; ++t) acc[4 + t] += *(const LDS f32x4*)(lds0 + BPART + chw * 3072 + t * 1024 + lane * 16);
        conv_stage<0, 4>(acc, aA, aB);
        stage4<4>(acc, lds0 + chw * CHB, lane);
    }
    __syncthreads();
    {
        u32 y[4];
#pragma unroll
        for (int c = 0; c < 4; ++c) y[c] = *(const LDS u32*)(lds0 + c * CHB + 4 * tid);
#pragma unroll
        for (int e = 0; e < 2; ++e) {
            u32 sel = e ? 0x07060302u : 0x05040100u;
            u32x2 o = {__builtin_amdgcn_perm(y[1], y[0], sel), __builtin_amdgcn_perm(y[3], y[2], sel)};
            *(u32x2*)(ob + (u32)(1024 + 2 * tid + e) * OROWB) = o;
        }
    }
}

extern "C" void kernel_launch(void* const* d_in, const int* in_sizes, int n_in,
                              void* d_out, int out_size, void* d_ws, size_t ws_size,
                              hipStream_t stream) {
    Params P;
    P.q = (const char*)d_in[0]; P.k = (const char*)d_in[1]; P.v = (const char*)d_in[2];
    P.shiftk = (const char*)d_in[3]; P.ssmk = (const char*)d_in[4]; P.ssmD = (const char*)d_in[5];
    P.u = (const char*)d_in[6]; P.Are = (const char*)d_in[7]; P.Aim = (const char*)d_in[8];
    P.Bre = (const char*)d_in[9]; P.Bim = (const char*)d_in[10]; P.Cre = (const char*)d_in[11];
    P.Cim = (const char*)d_in[12]; P.logdt = (const char*)d_in[13]; P.Dskip = (const char*)d_in[14];
    P.out = (char*)d_out;
    fused_kernel<<<256, 512, 0, stream>>>(P);
}
```

```cpp
#include <hip/hip_runtime.h>
#include <hip/hip_bf16.h>
#include <stdint.h>

typedef unsigned int u32;
typedef u32 u32x2 __attribute__((ext_vector_type(2)));
typedef u32 u32x4 __attribute__((ext_vector_type(4)));
typedef float f32x4 __attribute__((ext_vector_type(4)));
typedef float f32x16 __attribute__((ext_vector_type(16)));
typedef __bf16 bf16x8 __attribute__((ext_vector_type(8)));
typedef __bf16 bf16x2 __attribute__((ext_vector_type(2)));
typedef float f32x2 __attribute__((ext_vector_type(2)));
#define LDS __attribute__((address_space(3)))
#define MEMBAR() asm volatile("" ::: "memory")

constexpr int S_ = 2048, D_ = 640, N_ = 192;
constexpr int ROWB = D_ * 2;
constexpr int OROWB = 2 * D_ * 2;

constexpr int KC0 = 8;
constexpr int KC1 = KC0 + 4224;
constexpr int XR0 = 8400;
constexpr int XR1 = XR0 + 4640;
constexpr int CHB = XR1 + 4544;
constexpr int STG = 8 * CHB;
constexpr int FLG = STG + 8 * 2304;
constexpr int LDS_BYTES = FLG + 128;
constexpr int SCR = 70400;
constexpr int SCRB = 8192;
constexpr int TAB3 = SCR + 8 * SCRB;
static_assert(TAB3 + 4 * 2688 <= FLG && SCR >= 4 * CHB && SCR % 128 == 0, "lds");
static_assert(LDS_BYTES <= 163840, "lds");

struct Params {
    const char* q; const char* k; const char* v; const char* shiftk; const char* ssmk; const char* ssmD;
    const char* u; const char* Are; const char* Aim; const char* Bre; const char* Bim; const char* Cre;
    const char* Cim; const char* logdt; const char* Dskip; char* out;
};
#define BAR() do { asm volatile("s_waitcnt lgkmcnt(0)" ::: "memory"); __builtin_amdgcn_s_barrier(); MEMBAR(); } while (0)
#define CONSTP __attribute__((address_space(4)))
#define SCHED() __builtin_amdgcn_sched_barrier(0)

__device__ __forceinline__ float bflo(u32 x) { return __uint_as_float(x << 16); }
__device__ __forceinline__ float bfhi(u32 x) { return __uint_as_float(x & 0xffff0000u); }
__device__ __forceinline__ u32 pk(float lo, float hi) {
    f32x2 f = {lo, hi};
    bf16x2 b = __builtin_convertvector(f, bf16x2);
    return __builtin_bit_cast(u32, b);
}
__device__ __forceinline__ float mul1(float a, float b) { float r; asm("v_mul_f32 %0, %1, %2" : "=v"(r) : "v"(a), "v"(b)); return r; }
__device__ __forceinline__ u32 shr1(u32 old, u32 src) {
    return __builtin_amdgcn_update_dpp(old, src, 0x138, 0xf, 0xf, false);
}
__device__ __forceinline__ float ldbf(const char* p, int idx) {
    u32 w = *(const CONSTP u32*)(p + (idx & ~1) * 2);
    return (idx & 1) ? bfhi(w) : bflo(w);
}
__device__ __forceinline__ u32x2 rdH(u32 addr, int off) {
    u32x2 v = *(const LDS u32x2*)(addr + off);
    MEMBAR();
    return v;
}
__device__ __forceinline__ bf16x8 cat(u32x2 lo, u32x2 hi) {
    u32x4 v = {lo.x, lo.y, hi.x, hi.y};
    return __builtin_bit_cast(bf16x8, v);
}
__device__ __forceinline__ bf16x8 rdB(u32 addr, int off) {
    u32x4 v = *(const LDS u32x4*)(addr + off);
    MEMBAR();
    return __builtin_bit_cast(bf16x8, v);
}

typedef unsigned long long u64;
constexpr u64 rect(int s0, int s1, int t0, int t1) {
    u64 m = 0;
    for (int s = s0; s < s1; ++s) for (int t = t0; t < t1; ++t) if (s + t <= 7) m |= 1ull << (8 * s + t);
    return m;
}
constexpr bool used(u64 m, int s, int t) { return (m >> (8 * s + t)) & 1; }
struct Sched { int n, s[37], t[37], lo[37], hi[37], nb[37][8], first_a[37], first_b[37]; };
constexpr Sched make_sched(u64 m, bool dbl) {
    Sched r{};
    int n = 0;
    for (int s = 0; s < 8; ++s) for (int t = 0; t < 8; ++t) if (used(m, s, t)) { r.s[n] = s; r.t[n] = t; ++n; }
    r.n = n;
    bool sa[8] = {}, sb[8] = {}, done[8] = {};
    for (int i = 0; i <= n; ++i) { r.lo[i] = r.hi[i] = -1; r.first_a[i] = r.first_b[i] = 0; for (int t = 0; t < 8; ++t) r.nb[i][t] = 0; }
    for (int i = 0; i < n; ++i) {
        r.first_a[i] = !sa[r.s[i]]; sa[r.s[i]] = true;
        r.first_b[i] = !sb[r.t[i]]; sb[r.t[i]] = true;
        if (i + 1 == n || r.s[i + 1] != r.s[i]) { r.lo[i] = r.s[i]; r.hi[i + 1] = r.s[i]; }
    }
    for (int i = 0; i < n; ++i) {
        if (dbl) {
            if (r.lo[i] < 0 && r.hi[i] < 0)
                for (int t = 0; t < 8; ++t) if (sb[t] && !done[t]) { r.nb[i][t] = 1; done[t] = true; break; }
        } else {
            bool last = true;
            for (int k = i + 1; k < n; ++k) if (r.t[k] == r.t[i]) last = false;
            if (last) { r.nb[i][r.t[i]] = 1; done[r.t[i]] = true; }
        }
    }
    for (int t = 0; t < 8; ++t) if (sb[t] && !done[t]) r.nb[n][t] = 1;
    return r;
}
struct Frags { u32x2 al[8], ah[8]; bf16x8 b[2][8]; };
template <u64 M, bool DBL, int I, int T>
__device__ __forceinline__ void reload_b(Frags& f, u32 aB, int nxt, int on) {
    constexpr Sched S = make_sched(M, DBL);
    if constexpr (T < 8) {
        if constexpr (S.nb[I][T]) f.b[nxt][T] = rdB(aB, 512 * (7 - T) + on);
        reload_b<M, DBL, I, T + 1>(f, aB, nxt, on);
    }
}
template <u64 M, bool DBL, int RHO, bool MORE, int I>
__device__ __forceinline__ void conv_step(f32x4 (&acc)[8], Frags& f, u32 aA, u32 aB) {
    constexpr Sched S = make_sched(M, DBL);
    constexpr int cur = DBL ? RHO & 1 : 0, nxt = DBL ? (RHO + 1) & 1 : 0, on = 64 * (RHO + 1);
    if constexpr (I < S.n) {
        constexpr int s = S.s[I], t = S.t[I];
        acc[s + t] = __builtin_amdgcn_mfma_f32_16x16x32_bf16(cat(f.al[s], f.ah[s]), f.b[cur][t], acc[s + t], 0, 0, 0);
        SCHED();
    }
    if constexpr (MORE) {
        if constexpr (S.hi[I] >= 0) f.ah[S.hi[I]] = rdH(aA, 512 * S.hi[I] + on + 8);
        if constexpr (S.lo[I] >= 0) f.al[S.lo[I]] = rdH(aA, 512 * S.lo[I] + on);
        reload_b<M, DBL, I, 0>(f, aB, nxt, on);
        SCHED();
    }
    if constexpr (I < S.n) conv_step<M, DBL, RHO, MORE, I + 1>(acc, f, aA, aB);
}
template <u64 M, bool DBL, int I>
__device__ __forceinline__ void conv_first(Frags& f, u32 aA, u32 aB) {
    constexpr Sched S = make_sched(M, DBL);
    if constexpr (I < S.n) {
        if constexpr (S.first_b[I]) f.b[0][S.t[I]] = rdB(aB, 512 * (7 - S.t[I]));
        if constexpr (S.first_a[I]) { f.al[S.s[I]] = rdH(aA, 512 * S.s[I]); f.ah[S.s[I]] = rdH(aA, 512 * S.s[I] + 8); }
        conv_first<M, DBL, I + 1>(f, aA, aB);
    }
}
template <u64 M, bool DBL, int NR, int RHO>
__device__ __forceinline__ void conv_rho(f32x4 (&acc)[8], Frags& f, u32 aA, u32 aB) {
    if constexpr (RHO < NR) {
        conv_step<M, DBL, RHO, (RHO + 1 < NR), 0>(acc, f, aA, aB);
        conv_rho<M, DBL, NR, RHO + 1>(acc, f, aA, aB);
    }
}
template <u64 M, bool DBL, int NR, int PRIO>
__device__ __forceinline__ void conv_stage(f32x4 (&acc)[8], u32 aA, u32 aB) {
    asm volatile("" : "+v"(aA), "+v"(aB));
    __builtin_amdgcn_s_setprio(PRIO);
    Frags f;
    conv_first<M, DBL, 0>(f, aA, aB);
    SCHED();
    conv_rho<M, DBL, NR, 0>(acc, f, aA, aB);
#pragma unroll
    for (int t = 0; t < 8; ++t) asm volatile("" : "+v"(acc[t]));
}

template <int T0>
__device__ __forceinline__ void stage_bf16(const f32x4 (&acc)[8], u32 base, int lane) {
    int c = lane & 15, g = lane >> 4;
    u32 a = base + 4 * (18 * (c >> 1) + 4 * g + (c & 1));
#pragma unroll
    for (int t = 0; t < 2; ++t) {
        *(LDS u32*)(a + 576 * t) = pk(acc[T0 + t][0], acc[T0 + t][1]);
        *(LDS u32*)(a + 576 * t + 8) = pk(acc[T0 + t][2], acc[T0 + t][3]);
    }
}
__device__ __forceinline__ void post(u32 addr) {
    const u32 one = 1u;
    asm volatile("s_waitcnt lgkmcnt(0)\n\ts_mov_b64 exec, 1\n\tds_add_u32 %0, %1\n\ts_mov_b64 exec, -1" :: "v"(addr), "v"(one) : "memory");
}
template <int N, int PRIO, int SL = 4>
__device__ __forceinline__ void await(u32 addr) {
    __builtin_amdgcn_s_setprio(3);
    for (;;) {
        u32 f = *(volatile LDS u32*)addr;
        if (__builtin_amdgcn_readfirstlane(f) >= N) break;
        __builtin_amdgcn_s_sleep(SL);
    }
    __builtin_amdgcn_s_setprio(PRIO);
    MEMBAR();
}
#define FW(i) (lds0 + FLG + 4 * (i))
#define XHEAD FW(0)
#define KRDY2 FW(1)
#define XRDY FW(4)
#define LISS FW(6)
#define SDONE(q) FW(8 + (q))
#define TRDY(c) FW(12 + (c))
#define KRDY(c) FW(16 + (c))
#define KALL FW(2)

struct KV { u32x4 h[3]; u32x4 k[2]; u32x4 v[2]; };

__device__ __forceinline__ void load_halo(KV& r, const Params& P, int ch0, int blk) {
    const char* kb = P.k + ch0 * 2;
#pragma unroll
    for (int e = 0; e < 3; ++e) {
        int hr = 128 * blk - 3 + e;
        r.h[e] = *(const u32x4*)(kb + (hr < 0 ? 0 : hr) * ROWB);
    }
}
__device__ __forceinline__ u32x4 load_row(const char* base, int ch0, int row) {
    return *(const u32x4*)(base + ch0 * 2 + (u32)row * ROWB);
}

__device__ __forceinline__ void make_s(const KV& r, const u32 (&sw)[8][4], bool first, u32 (&sv)[4][4]) {
    const u32 ones = 0x3f803f80u;
#pragma unroll
    for (int d = 0; d < 4; ++d) {
        u32 k3 = r.k[0][d], k4 = r.k[1][d];
        u32 k1 = shr1(first ? 0u : r.h[1][d], k3);
        u32 k2 = shr1(first ? 0u : r.h[2][d], k4);
        u32 k0 = shr1(first ? 0u : r.h[0][d], k2);
        u32 b_0, b_1, b_2, b_3, c_0, c_1, c_2, c_3;
        asm("v_dot2_f32_bf16 %0, %12, %20, 0\n\tv_dot2_f32_bf16 %1, %12, %24, 0\n\tv_dot2_f32_bf16 %2, %13, %20, 0\n\tv_dot2_f32_bf16 %3, %13, %24, 0\n\t"
            "v_dot2_f32_bf16 %4, %13, %19, 0\n\tv_dot2_f32_bf16 %5, %13, %23, 0\n\tv_dot2_f32_bf16 %6, %14, %19, 0\n\tv_dot2_f32_bf16 %7, %14, %23, 0\n\t"
            "v_dot2_f32_bf16 %8, %14, %18, 0\n\tv_dot2_f32_bf16 %9, %14, %22, 0\n\tv_dot2_f32_bf16 %10, %15, %18, 0\n\tv_dot2_f32_bf16 %11, %15, %22, 0\n\t"
            "v_cvt_pk_bf16_f32 %0, %0, %4\n\tv_cvt_pk_bf16_f32 %1, %1, %5\n\tv_cvt_pk_bf16_f32 %2, %2, %6\n\tv_cvt_pk_bf16_f32 %3, %3, %7\n\t"
            "v_dot2_f32_bf16 %4, %15, %17, 0\n\tv_dot2_f32_bf16 %5, %15, %21, 0\n\tv_dot2_f32_bf16 %6, %16, %17, 0\n\tv_dot2_f32_bf16 %7, %16, %21, 0\n\t"
            "v_dot2_f32_bf16 %0, %0, %25, 0\n\tv_dot2_f32_bf16 %1, %1, %25, 0\n\tv_dot2_f32_bf16 %2, %2, %25, 0\n\tv_dot2_f32_bf16 %3, %3, %25, 0\n\t"
            "v_cvt_pk_bf16_f32 %0, %0, %8\n\tv_cvt_pk_bf16_f32 %1, %1, %9\n\tv_cvt_pk_bf16_f32 %2, %2, %10\n\tv_cvt_pk_bf16_f32 %3, %3, %11\n\t"
            "v_dot2_f32_bf16 %0, %0, %25, 0\n\tv_dot2_f32_bf16 %1, %1, %25, 0\n\tv_dot2_f32_bf16 %2, %2, %25, 0\n\tv_dot2_f32_bf16 %3, %3, %25, 0\n\t"
            "v_cvt_pk_bf16_f32 %0, %0, %4\n\tv_cvt_pk_bf16_f32 %1, %1, %5\n\tv_cvt_pk_bf16_f32 %2, %2, %6\n\tv_cvt_pk_bf16_f32 %3, %3, %7\n\t"
            "v_dot2_f32_bf16 %0, %0, %25, 0\n\tv_dot2_f32_bf16 %1, %1, %25, 0\n\tv_dot2_f32_bf16 %2, %2, %25, 0\n\tv_dot2_f32_bf16 %3, %3, %25, 0\n\t"
            "v_cvt_pk_bf16_f32 %0, %0, 0\n\tv_cvt_pk_bf16_f32 %1, 0, %1\n\tv_cvt_pk_bf16_f32 %2, %2, 0\n\tv_cvt_pk_bf16_f32 %3, 0, %3"
            : "=&v"(sv[d][0]), "=&v"(sv[d][1]), "=&v"(sv[d][2]), "=&v"(sv[d][3]), "=&v"(b_0), "=&v"(b_1), "=&v"(b_2), "=&v"(b_3),
              "=&v"(c_0), "=&v"(c_1), "=&v"(c_2), "=&v"(c_3)
            : "v"(k0), "v"(k1), "v"(k2), "v"(k3), "v"(k4),
              "s"(sw[2 * d][0]), "s"(sw[2 * d][1]), "s"(sw[2 * d][2]), "s"(sw[2 * d][3]),
              "s"(sw[2 * d + 1][0]), "s"(sw[2 * d + 1][1]), "s"(sw[2 * d + 1][2]), "s"(sw[2 * d + 1][3]), "s"(ones));
    }
}
__device__ __forceinline__ void finish_x(const KV& r, const u32 (&sv)[4][4], u32 lds0, int blk, int lane) {
    int L = 64 * blk + lane;
    u32 a1 = lds0 + XR1 + 4092 - 4 * L;
    u32 a0 = lds0 + XR0 + 4088 - 4 * L;
#pragma unroll
    for (int d = 0; d < 4; ++d) {
        u32 o0, o1, t2, t3;
        asm("v_dot2_f32_bf16 %0, %4, %8, 0\n\tv_dot2_f32_bf16 %2, %6, %9, 0\n\tv_dot2_f32_bf16 %1, %5, %8, 0\n\tv_dot2_f32_bf16 %3, %7, %9, 0\n\t"
            "s_nop 1\n\t"
            "v_cvt_pk_bf16_f32 %0, %2, %0\n\tv_cvt_pk_bf16_f32 %1, %3, %1"
            : "=&v"(o0), "=&v"(o1), "=&v"(t2), "=&v"(t3)
            : "v"(sv[d][0]), "v"(sv[d][1]), "v"(sv[d][2]), "v"(sv[d][3]), "v"(r.v[0][d]), "v"(r.v[1][d]));
        *(LDS u32*)(a1 + (2 * d) * CHB) = o0;
        *(LDS u32*)(a0 + (2 * d) * CHB) = o0;
        *(LDS u32*)(a1 + (2 * d + 1) * CHB) = o1;
        *(LDS u32*)(a0 + (2 * d + 1) * CHB) = o1;
    }
}

__device__ __forceinline__ void make_x_plain(const KV& r, const u32 (&sw)[8][4], u32 lds0, int blk, int lane) {
    int L = 64 * blk + lane;
    u32 a1 = lds0 + XR1 + 4092 - 4 * L;
    u32 a0 = lds0 + XR0 + 4088 - 4 * L;
#pragma unroll
    for (int d = 0; d < 4; ++d) {
        u32 k3 = r.k[0][d], k4 = r.k[1][d];
        u32 k1 = shr1(r.h[1][d], k3);
        u32 k2 = shr1(r.h[2][d], k4);
        u32 k0 = shr1(r.h[0][d], k2);
        SCHED();
        u32 o0, o1, f0, f1, f2, f3, f4, ta, tb, tc, td;
        asm(
            "v_lshlrev_b32 %2, 16, %11\n\tv_lshlrev_b32 %3, 16, %12\n\tv_lshlrev_b32 %4, 16, %13\n\tv_lshlrev_b32 %5, 16, %14\n\t"
            "v_lshlrev_b32 %6, 16, %15\n\tv_mul_f32 %7, %21, %2\n\tv_mul_f32 %8, %20, %3\n\tv_mul_f32 %9, %21, %3\n\t"
            "v_mul_f32 %10, %20, %4\n\tv_cvt_pk_bf16_f32 %7, 0, %7\n\tv_cvt_pk_bf16_f32 %8, 0, %8\n\tv_cvt_pk_bf16_f32 %9, 0, %9\n\t"
            "v_cvt_pk_bf16_f32 %10, 0, %10\n\tv_add_f32 %7, %7, %8\n\tv_add_f32 %9, %9, %10\n\tv_mul_f32 %8, %19, %4\n\t"
            "v_mul_f32 %10, %19, %5\n\tv_cvt_pk_bf16_f32 %7, 0, %7\n\tv_cvt_pk_bf16_f32 %9, 0, %9\n\tv_cvt_pk_bf16_f32 %8, 0, %8\n\t"
            "v_cvt_pk_bf16_f32 %10, 0, %10\n\tv_add_f32 %7, %7, %8\n\tv_add_f32 %9, %9, %10\n\tv_mul_f32 %8, %18, %5\n\t"
            "v_mul_f32 %10, %18, %6\n\tv_cvt_pk_bf16_f32 %7, 0, %7\n\tv_cvt_pk_bf16_f32 %9, 0, %9\n\tv_cvt_pk_bf16_f32 %8, 0, %8\n\t"
            "v_cvt_pk_bf16_f32 %10, 0, %10\n\tv_add_f32 %7, %7, %8\n\tv_add_f32 %9, %9, %10\n\tv_lshlrev_b32 %8, 16, %16\n\t"
            "v_lshlrev_b32 %10, 16, %17\n\tv_cvt_pk_bf16_f32 %7, 0, %7\n\tv_cvt_pk_bf16_f32 %9, 0, %9\n\tv_mul_f32 %7, %7, %8\n\t"
            "v_mul_f32 %9, %9, %10\n\tv_cvt_pk_bf16_f32 %0, %9, %7\n\tv_and_b32 %2, 0xffff0000, %11\n\tv_and_b32 %3, 0xffff0000, %12\n\t"
            "v_and_b32 %4, 0xffff0000, %13\n\tv_and_b32 %5, 0xffff0000, %14\n\tv_and_b32 %6, 0xffff0000, %15\n\tv_mul_f32 %7, %25, %2\n\t"
            "v_mul_f32 %8, %24, %3\n\tv_mul_f32 %9, %25, %3\n\tv_mul_f32 %10, %24, %4\n\tv_cvt_pk_bf16_f32 %7, 0, %7\n\t"
            "v_cvt_pk_bf16_f32 %8, 0, %8\n\tv_cvt_pk_bf16_f32 %9, 0, %9\n\tv_cvt_pk_bf16_f32 %10, 0, %10\n\tv_add_f32 %7, %7, %8\n\t"
            "v_add_f32 %9, %9, %10\n\tv_mul_f32 %8, %23, %4\n\tv_mul_f32 %10, %23, %5\n\tv_cvt_pk_bf16_f32 %7, 0, %7\n\t"
            "v_cvt_pk_bf16_f32 %9, 0, %9\n\tv_cvt_pk_bf16_f32 %8, 0, %8\n\tv_cvt_pk_bf16_f32 %10, 0, %10\n\tv_add_f32 %7, %7, %8\n\t"
            "v_add_f32 %9, %9, %10\n\tv_mul_f32 %8, %22, %5\n\tv_mul_f32 %10, %22, %6\n\tv_cvt_pk_bf16_f32 %7, 0, %7\n\t"
            "v_cvt_pk_bf16_f32 %9, 0, %9\n\tv_cvt_pk_bf16_f32 %8, 0, %8\n\tv_cvt_pk_bf16_f32 %10, 0, %10\n\tv_add_f32 %7, %7, %8\n\t"
            "v_add_f32 %9, %9, %10\n\tv_and_b32 %8, 0xffff0000, %16\n\tv_and_b32 %10, 0xffff0000, %17\n\tv_cvt_pk_bf16_f32 %7, 0, %7\n\t"
            "v_cvt_pk_bf16_f32 %9, 0, %9\n\tv_mul_f32 %7, %7, %8\n\tv_mul_f32 %9, %9, %10\n\tv_cvt_pk_bf16_f32 %1, %9, %7"
            : "=&v"(o0), "=&v"(o1), "=&v"(f0), "=&v"(f1), "=&v"(f2), "=&v"(f3), "=&v"(f4), "=&v"(ta), "=&v"(tb), "=&v"(tc), "=&v"(td)
            : "v"(k0), "v"(k1), "v"(k2), "v"(k3), "v"(k4), "v"(r.v[0][d]), "v"(r.v[1][d]),
              "s"(sw[2 * d][0] << 16), "s"(sw[2 * d][1] << 16), "s"(sw[2 * d][2] << 16), "s"(sw[2 * d][3] << 16),
              "s"(sw[2 * d + 1][0]), "s"(sw[2 * d + 1][1]), "s"(sw[2 * d + 1][2]), "s"(sw[2 * d + 1][3]));
        *(LDS u32*)(a1 + (2 * d) * CHB) = o0;
        *(LDS u32*)(a0 + (2 * d) * CHB) = o0;
        *(LDS u32*)(a1 + (2 * d + 1) * CHB) = o1;
        *(LDS u32*)(a0 + (2 * d + 1) * CHB) = o1;
    }
}

__device__ __forceinline__ void zero_pads(u32 lds0, int c0, int nch, int t) {
    if (t < 253) {
        u32 off;
        if (t < 14) off = KC0 + 4 * t;
        else if (t < 28) off = KC1 + 4 * (t - 14);
        else if (t < 141) off = XR0 + 4092 + 4 * (t - 28);
        else off = XR1 + 4096 + 4 * (t - 141);
        for (int c = 0; c < nch; ++c) *(LDS u32*)(lds0 + (c0 + c) * CHB + off) = 0u;
    }
}

__device__ __forceinline__ f32x2 cexp(float lm, float rev) {
    float m = __builtin_amdgcn_exp2f(lm);
    float r = __builtin_amdgcn_fractf(rev);
    return f32x2{m * __builtin_amdgcn_cosf(r), m * __builtin_amdgcn_sinf(r)};
}
#define PKM(d, a, b) "v_pk_mul_f32 " d ", " a ", " b " op_sel_hi:[0,1]\n\t"
#define PKF(d, a, b) "v_pk_fma_f32 " d ", " a ", " b ", " d " op_sel:[1,1,0] op_sel_hi:[1,0,1] neg_lo:[0,1,0]\n\t"
__device__ __forceinline__ f32x2 cmul(f32x2 s, f32x2 g) {
    f32x2 t;
    asm(PKM("%0", "%1", "%2") PKF("%0", "%1", "%2") : "=&v"(t) : "v"(s), "v"(g));
    return t;
}
__device__ __forceinline__ void cmul4(f32x2 (&s)[4], f32x2 g0, f32x2 g1, f32x2 g2) {
    f32x2 t0, t1, t2, t3;
    asm(PKM("%0", "%4", "%8") PKM("%1", "%5", "%9") PKM("%2", "%6", "%10") PKM("%3", "%7", "%10")
        PKF("%0", "%4", "%8") PKF("%1", "%5", "%9") PKF("%2", "%6", "%10") PKF("%3", "%7", "%10")
        : "=&v"(t0), "=&v"(t1), "=&v"(t2), "=&v"(t3) : "v"(s[0]), "v"(s[1]), "v"(s[2]), "v"(s[3]), "v"(g0), "v"(g1), "v"(g2));
    s[0] = t0; s[1] = t1; s[2] = t2; s[3] = t3;
}
__device__ __forceinline__ f32x2 sel(bool c, f32x2 a, f32x2 b) { return f32x2{c ? a.x : b.x, c ? a.y : b.y}; }

__device__ __forceinline__ u32x4 gate_row(const u32 (&y)[8], bool hi, u32x4 q) {
    u32x4 o;
#pragma unroll
    for (int d = 0; d < 4; ++d) {
        float fa = hi ? bfhi(y[2 * d]) : bflo(y[2 * d]), fb = hi ? bfhi(y[2 * d + 1]) : bflo(y[2 * d + 1]);
        o[d] = pk(mul1(fa, bflo(q[d])), mul1(fb, bfhi(q[d])));
    }
    return o;
}

struct Warm { const char* base[3]; u32 off[3]; };
__device__ __forceinline__ void warm_prep(Warm& w, const Params& P, bool odd, int chA, int chB, int pw, int lane) {
    const int nw = odd ? 192 : 160;
    const int iA = odd ? 64 : 98, iB = odd ? 98 : 64, nch = odd ? 64 : 96;
    const int l3 = (lane * 43) >> 7, s3 = lane - 3 * l3;
    const int lrA = odd ? lane >> 1 : l3, lsA = odd ? lane & 1 : s3, nrA = odd ? 32 : 21;
    const int lrB = odd ? l3 : lane >> 1, lsB = odd ? s3 : lane & 1, nrB = odd ? 21 : 32;
#pragma unroll
    for (int i = 0; i < 3; ++i) {
        const int it = pw + nw * i;
        const int c1 = it - iA, c2 = c1 - nch, c3 = c2 - iB, c4 = c3 - iA;
        const bool isK = c1 >= 0 && c2 < 0, isU = c2 >= 0 && c3 < 0, isQ = c4 >= 0;
        const int kvi = c1 < 0 ? it : c3 + iA;
        const char* kv = (kvi & 1) ? P.v : P.k;
        w.base[i] = isK ? P.ssmk : (isU ? P.u : (isQ ? P.q : kv));
        const int rb = isU ? c2 : (isQ ? c4 : kvi >> 1);
        const int row = min(rb * (isU ? nrB : nrA) + (isU ? lrB : lrA), 2047);
        const u32 col = isU ? chB * 2 + lsB * 64 : chA * 2 + lsA * 64;
        w.off[i] = isK ? (u32)((chA + c1) * (S_ * 2) + lane * 64) : (u32)(row * ROWB) + col;
    }
}
__device__ __forceinline__ void warm_issue(u32 (&pf)[3], const Warm& w) {
#pragma unroll
    for (int i = 0; i < 3; ++i) pf[i] = *(const u32*)(w.base[i] + w.off[i]);
}

__device__ __forceinline__ void load_kv(KV& r, const Params& P, int ch0, int blk, int lane) {
    load_halo(r, P, ch0, blk);
#pragma unroll
    for (int e = 0; e < 2; ++e) r.k[e] = load_row(P.k, ch0, 128 * blk + 2 * lane + e);
#pragma unroll
    for (int e = 0; e < 2; ++e) r.v[e] = load_row(P.v, ch0, 128 * blk + 2 * lane + e);
    SCHED();
}

__global__ __launch_bounds__(1024, 4) void fused_kernel(Params P) {
    __shared__ __attribute__((aligned(128))) char smem[LDS_BYTES];
    const u32 lds0 = (u32)(size_t)(LDS char*)smem;
    const int tid = threadIdx.x, lane = tid & 63;
    int wave = __builtin_amdgcn_readfirstlane(tid >> 6);
    const int g8 = blockIdx.x & 7;
    int j = blockIdx.x >> 3;
    const bool odd = g8 & 1;
    const int nA = odd ? 8 : 12, nB = odd ? 24 : 16;
    const int chA = 160 * (g8 >> 1) + (odd ? 96 : 0), chB = 160 * (g8 >> 1) + (odd ? 64 : 0);
    if (tid < 32) *(LDS u32*)FW(tid) = 0u;
    asm volatile("" :: "s"(P.q), "s"(P.k), "s"(P.v), "s"(P.shiftk), "s"(P.ssmk), "s"(P.ssmD), "s"(P.u), "s"(P.Are));
    asm volatile("" :: "s"(P.Aim), "s"(P.Bre), "s"(P.Bim), "s"(P.Cre), "s"(P.Cim), "s"(P.logdt), "s"(P.Dskip), "s"(P.out));
    const int mw = wave, pw = wave & 3, pt = tid & 255;
    u32x4 fst[5];
    u32 pr[6];
    if (j < nA) {
        const int ch0 = chA + 8 * j;
        if (wave < 8) {
            __builtin_amdgcn_s_setprio(3);
#pragma unroll
            for (int e = 0; e < 3; ++e) fst[e] = load_row(P.k, ch0, max(128 * wave - 3 + e, 0));
#pragma unroll
            for (int e = 0; e < 2; ++e) fst[3 + e] = load_row(P.k, ch0, 2 * tid + e);
        } else if (wave >= 12) {
            __builtin_amdgcn_s_setprio(2);
#pragma unroll
            for (int i = 0; i < 4; ++i) fst[i] = *(const u32x4*)(P.ssmk + (size_t)(ch0 + 2 * pw + (i >> 1)) * (S_ * 2) + ((i & 1) * 64 + lane) * 16);
        }
    } else if (j < nA + nB && wave < 12) {
        const u32 o = ((chB + 4 * (j - nA) + pw) * N_ + 64 * (wave >> 2) + 2 * (lane & 31)) * 2;
        pr[0] = *(const u32*)(P.Are + o); pr[1] = *(const u32*)(P.Aim + o); pr[2] = *(const u32*)(P.Bre + o);
        pr[3] = *(const u32*)(P.Bim + o); pr[4] = *(const u32*)(P.Cre + o); pr[5] = *(const u32*)(P.Cim + o);
    }
    SCHED();
    asm volatile("" : "+s"(j), "+s"(wave));
    const int fr = lane & 15, fg = lane >> 4;
    const u32 relA = ((fr & 1) ? KC1 : KC0) + 8 * (fr >> 1) + 16 * fg;
    const u32 relB = ((fr & 1) ? XR1 : XR0) + 4032 - 3584 - 64 * (fr >> 1) + 16 * fg;

    if (j < nA) {
        const int ch0 = chA + 8 * j;
        u32 sw[8][4];
        auto load_sw = [&]() {
            const CONSTP u32* sp = (const CONSTP u32*)(P.shiftk + ch0 * 8);
#pragma unroll
            for (int c = 0; c < 8; ++c) {
                u32 a = sp[2 * c], b = sp[2 * c + 1];
                if (c & 1) { sw[c][0] = a << 16; sw[c][1] = a & 0xffff0000u; sw[c][2] = b << 16; sw[c][3] = b & 0xffff0000u; }
                else { sw[c][0] = a & 0xffffu; sw[c][1] = a >> 16; sw[c][2] = b & 0xffffu; sw[c][3] = b >> 16; }
            }
        };
        u32x4 kc[2][2];
        u32 pv[2];
        auto load_k = [&](int half) {
#pragma unroll
            for (int c = 0; c < 2; ++c)
#pragma unroll
                for (int i = 0; i < 2; ++i) kc[c][i] = *(const u32x4*)(P.ssmk + (size_t)(ch0 + 2 * pw + c) * (S_ * 2) + ((2 * half + i) * 64 + lane) * 16);
            SCHED();
        };
        auto copy_k = [&](int half) {
#pragma unroll
            for (int c = 0; c < 2; ++c)
#pragma unroll
                for (int i = 0; i < 2; ++i) {
                    u32x4 w = kc[c][i];
                    u32 pm = shr1(pv[c], w.w);
                    pv[c] = __builtin_amdgcn_readlane(w.w, 63);
                    u32x4 s;
                    s.x = __builtin_amdgcn_alignbit(w.x, pm, 16);
                    s.y = __builtin_amdgcn_alignbit(w.y, w.x, 16);
                    s.z = __builtin_amdgcn_alignbit(w.z, w.y, 16);
                    s.w = __builtin_amdgcn_alignbit(w.w, w.z, 16);
                    u32 a = lds0 + (2 * pw + c) * CHB + 56 + ((2 * half + i) * 64 + lane) * 16;
                    *(LDS u32x4*)(a + KC1) = w;
                    *(LDS u32x4*)(a + KC0) = s;
                }
        };
        auto load_q = [&](u32x4 (&qq)[2], int quarter) {
#pragma unroll
            for (int e = 0; e < 2; ++e) qq[e] = load_row(P.q, ch0, 512 * quarter + 2 * pt + e);
            SCHED();
        };
        auto epi = [&](int quarter, const u32x4 (&qq)[2]) {
            u32 y[8];
            const u32 a = lds0 + (quarter < 2 ? STG + 1152 * quarter : 2304 + 1152 * (quarter - 2)) + 4 * pt + 8 * (pt >> 4);
#pragma unroll
            for (int c = 0; c < 8; ++c) y[c] = *(const LDS u32*)(a + c * (quarter < 2 ? 2304 : CHB));
#pragma unroll
            for (int e = 0; e < 2; ++e) *(u32x4*)(P.out + ch0 * 2 + (u32)(512 * quarter + 2 * pt + e) * OROWB) = gate_row(y, e, qq[e]);
            SCHED();
        };
        auto epi_last = [&](int e, u32x4 qq) {
            await<8, 3, 1>(SDONE(3));
            u32 y[8];
            const u32 a = lds0 + 2304 + 1152 + 4 * pt + 8 * (pt >> 4);
#pragma unroll
            for (int c = 0; c < 8; ++c) y[c] = *(const LDS u32*)(a + c * CHB);
            *(u32x4*)(P.out + ch0 * 2 + (u32)(1536 + 2 * pt + e) * OROWB) = gate_row(y, e, qq);
        };
        if (wave >= 12) {
            kc[0][0] = fst[0]; kc[0][1] = fst[1]; kc[1][0] = fst[2]; kc[1][1] = fst[3];
            BAR();
            zero_pads(lds0, 0, 8, pt);
            pv[0] = pv[1] = 0u;
#pragma unroll
            for (int c = 0; c < 2; ++c) {
                const float dsk = ldbf(P.ssmD, ch0 + 2 * pw + c);
                if (lane == 0) {
                    u32 d0 = kc[c][0].x;
                    kc[c][0].x = (pk(bflo(d0) + dsk, 0.f) & 0xffffu) | (d0 & 0xffff0000u);
                }
            }
            copy_k(0);
            post(XHEAD);
            __builtin_amdgcn_s_setprio(1);
            load_k(1);
            __builtin_amdgcn_s_setprio(3);
            u32x4 qa[2], qb[2], qc[2];
            copy_k(1);
            post(KRDY2);
            await<4, 0>(LISS);
            load_q(qa, 0);
            load_q(qb, 1);
            load_q(qc, 2);
            const u32x4 ql = load_row(P.q, ch0, 1537 + 2 * pt);
            SCHED();
            await<8, 3>(SDONE(0));
            epi(0, qa);
            epi(1, qb);
            await<8, 3, 2>(SDONE(2));
            epi(2, qc);
            epi_last(1, ql);
        } else if (wave >= 8) {
            KV ra, rb;
            load_sw();
            BAR();
            __builtin_amdgcn_s_sleep(32);
            load_kv(ra, P, ch0, 8 + pw, lane);
            load_kv(rb, P, ch0, 12 + pw, lane);
            post(LISS);
            __builtin_amdgcn_s_setprio(3);
            make_x_plain(ra, sw, lds0, 8 + pw, lane);
            make_x_plain(rb, sw, lds0, 12 + pw, lane);
            post(XRDY);
            epi_last(0, load_row(P.q, ch0, 1536 + 2 * pt));
        } else {
            {
                KV r;
                u32 sv[4][4];
                r.h[0] = fst[0]; r.h[1] = fst[1]; r.h[2] = fst[2]; r.k[0] = fst[3]; r.k[1] = fst[4];
                BAR();
                __builtin_amdgcn_s_setprio(1);
#pragma unroll
                for (int e = 0; e < 2; ++e) r.v[e] = load_row(P.v, ch0, 2 * tid + e);
                SCHED();
                load_sw();
                make_s(r, sw, mw == 0, sv);
                finish_x(r, sv, lds0, mw, lane);
                post(XHEAD);
            }
            f32x4 acc[8];
#pragma unroll
            for (int t = 0; t < 8; ++t) acc[t] = f32x4{0.f, 0.f, 0.f, 0.f};
            const u32 aA = lds0 + mw * CHB + relA, aB = lds0 + mw * CHB + relB;
            await<12, 0, 1>(XHEAD);
            conv_stage<rect(0, 4, 0, 4), true, 8, 3>(acc, aA, aB);
            stage_bf16<0>(acc, lds0 + STG + mw * 2304, lane);
            stage_bf16<2>(acc, lds0 + STG + mw * 2304 + 1152, lane);
            post(SDONE(0));
            await<4, 0>(KRDY2);
            conv_stage<rect(4, 8, 0, 4), true, 8, 2>(acc, aA, aB);
            await<4, 0>(XRDY);
            conv_stage<rect(0, 8, 4, 6), true, 8, 1>(acc, aA, aB);
            stage_bf16<4>(acc, lds0 + mw * CHB + 2304, lane);
            post(SDONE(2));
            conv_stage<rect(0, 8, 6, 8), true, 8, 0>(acc, aA, aB);
            stage_bf16<6>(acc, lds0 + mw * CHB + 2304 + 1152, lane);
            post(SDONE(3));
        }
        return;
    }

    if (j >= nA + nB) {
        if (wave < 8) {
            Warm wm;
            u32 pf[3];
            warm_prep(wm, P, odd, chA, chB, (j - nA) * 8 + wave, lane);
            warm_issue(pf, wm);
            asm volatile("" :: "v"(pf[0]), "v"(pf[1]), "v"(pf[2]));
        }
        return;
    }
    const int ch0 = chB + 4 * (j - nA);
    auto epiB = [&](int q) {
        char* ob = P.out + D_ * 2 + ch0 * 2;
        u32 y[4];
        const u32 a = lds0 + SCR + q * 1152 + 4 * pt + 8 * (pt >> 4);
#pragma unroll
        for (int c = 0; c < 4; ++c) y[c] = *(const LDS u32*)(a + c * 4608);
#pragma unroll
        for (int e = 0; e < 2; ++e) {
            const u32 sl = e ? 0x07060302u : 0x05040100u;
            *(u32x2*)(ob + (u32)(512 * q + 2 * pt + e) * OROWB) = u32x2{__builtin_amdgcn_perm(y[1], y[0], sl), __builtin_amdgcn_perm(y[3], y[2], sl)};
        }
    };
    if (wave >= 12) {
        __builtin_amdgcn_s_setprio(3);
        BAR();
        Warm wm, wm2;
        warm_prep(wm, P, odd, chA, chB, (j - nA) * 8 + 2 * pw, lane);
        warm_prep(wm2, P, odd, chA, chB, (j - nA) * 8 + 2 * pw + 1, lane);
        const u32 pf0 = *(const u32*)(wm.base[1] + wm.off[1]), pf1 = *(const u32*)(wm2.base[1] + wm2.off[1]);
        const u32 pf2 = *(const u32*)(wm.base[2] + wm.off[2]), pf3 = *(const u32*)(wm2.base[2] + wm2.off[2]);
        SCHED();
        if (pt < 225) {
            const u32 off = pt < 113 ? XR0 + 4092 + 4 * pt : XR1 + 4096 + 4 * (pt - 113);
#pragma unroll
            for (int c = 0; c < 4; ++c) *(LDS u32*)(lds0 + c * CHB + off) = 0u;
        }
        asm volatile("" :: "v"(pf0), "v"(pf1), "v"(pf2), "v"(pf3));
        __builtin_amdgcn_s_sleep(32);
        u32x2 ur[2][4];
#pragma unroll
        for (int h = 0; h < 2; ++h)
#pragma unroll
            for (int e = 0; e < 4; ++e) ur[h][e] = *(const u32x2*)(P.u + ch0 * 2 + (u32)(1024 * h + 4 * pt + e) * ROWB);
#pragma unroll
        for (int h = 0; h < 2; ++h) {
            u32 a1 = lds0 + XR1 + 4088 - 2048 * h - 8 * pt;
            u32 a0 = lds0 + XR0 + 4084 - 2048 * h - 8 * pt;
#pragma unroll
            for (int c = 0; c < 4; ++c) {
                u32 sl = (c & 1) ? 0x03020706u : 0x01000504u;
                u32 d0 = __builtin_amdgcn_perm(ur[h][3][c >> 1], ur[h][2][c >> 1], sl);
                u32 d1 = __builtin_amdgcn_perm(ur[h][1][c >> 1], ur[h][0][c >> 1], sl);
                *(LDS u32x2*)(a1 + c * CHB) = u32x2{d0, d1};
                *(LDS u32*)(a0 + c * CHB) = d0;
                *(LDS u32*)(a0 + c * CHB + 4) = d1;
            }
        }
        {
            const u32 one = 1u, ad = KRDY(lane & 3);
            asm volatile("s_waitcnt lgkmcnt(0)\n\ts_mov_b64 exec, 15\n\tds_add_u32 %0, %1\n\ts_mov_b64 exec, -1" :: "v"(ad), "v"(one) : "memory");
        }
        await<4, 3>(SDONE(0));
        epiB(0);
        epiB(1);
        await<4, 3>(SDONE(2));
        epiB(2);
        return;
    }
    const int chw = wave & 3, part = wave >> 2;
    u32 pf0 = 0;
    if (wave < 8) {
        Warm wm;
        warm_prep(wm, P, odd, chA, chB, (j - nA) * 8 + wave, lane);
        pf0 = *(const u32*)(wm.base[0] + wm.off[0]);
    }
    SCHED();
    BAR();
    const float dsk = ldbf(P.Dskip, ch0 + chw);
    const float dt = bflo(pk(expf(ldbf(P.logdt, ch0 + chw)), 0.f));
    const bool rows = lane >= 32;
    {
        const u32 scr = part < 2 ? lds0 + SCR + wave * SCRB : ((lds0 + chw * CHB + 127) & ~127u);
        f32x16 kacc;
#pragma unroll
        for (int i = 0; i < 16; ++i) kacc[i] = 0.f;
        u32 wa[2][8], ra[4];
#pragma unroll
        for (int c = 0; c < 8; ++c) {
            wa[0][c] = (scr + (rows ? 4096 : 0) + 4 * (lane & 31)) ^ (c << 4);
            wa[1][c] = wa[0][c] + 128;
            asm volatile("" : "+v"(wa[0][c]), "+v"(wa[1][c]));
        }
#pragma unroll
        for (int ks = 0; ks < 4; ++ks) ra[ks] = scr + 128 * (lane & 31) + ((32 * ks + 16 * (lane >> 5)) ^ (((lane >> 1) & 7) << 4));
        asm volatile("" : "+v"(ra[0]), "+v"(ra[1]), "+v"(ra[2]), "+v"(ra[3]));
        const float pm1 = (rows ? 1.f : 8.f) * 1.4426950408889634f, pm2 = (rows ? 256.f : 64.f) * 1.4426950408889634f;
        const float pv1 = (rows ? -1.f : 8.f) * 0.15915494309189535f, pv2 = (rows ? -256.f : 64.f) * 0.15915494309189535f;
        __builtin_amdgcn_s_setprio(1);
        const float cm1 = dt * pm1, cm2 = dt * pm2, cv1 = dt * pv1, cv2 = dt * pv2;
#pragma unroll
        for (int t = 0; t < 2; ++t) {
#define PRV(a) (t ? bfhi(pr[a]) : bflo(pr[a]))
            const float ar = PRV(0), ai = PRV(1), br = PRV(2), bi = PRV(3), cr = PRV(4), ci = PRV(5);
            f32x2 s[4];
            s[0] = f32x2{rows ? cr * br - ci * bi : 1.f, rows ? -(cr * bi + ci * br) : 0.f};
            const f32x2 P1 = cexp(ar * cm1, ai * cv1), P2 = cexp(ar * cm2, ai * cv2);
            const f32x2 P4 = cmul(P1, P1), P5 = cmul(P4, P4);
            s[1] = cmul(s[0], P2);
            s[2] = cmul(s[1], P2);
            s[3] = cmul(s[2], sel(rows, P5, P2));
            const f32x2 g1 = sel(rows, P4, P1), g2 = sel(rows, P2, P1);
#pragma unroll
            for (int k = 0; k < 8; ++k) {
#pragma unroll
                for (int qb = 0; qb < 2; ++qb) {
                    const int c = 4 * qb + (k >> 1);
                    *(LDS u32*)(wa[k & 1][c] + 256 * c) = pk(s[qb].x, s[qb].y);
                    *(LDS u32*)(wa[k & 1][c] + 256 * c + 2048) = pk(s[qb + 2].x, s[qb + 2].y);
                }
                if (k < 7) cmul4(s, P1, g1, g2);
            }
#pragma unroll
            for (int ks = 0; ks < 4; ++ks) {
                u32x4 av = *(const LDS u32x4*)(ra[ks] + 4096);
                u32x4 bv = *(const LDS u32x4*)(ra[ks]);
                kacc = __builtin_amdgcn_mfma_f32_32x32x16_bf16(__builtin_bit_cast(bf16x8, av), __builtin_bit_cast(bf16x8, bv), kacc, 0, 0, 0);
            }
            MEMBAR();
        }
        const int col = lane & 31, h = lane >> 5;
        const u32 tb = part < 2 ? scr : lds0 + TAB3 + chw * 2688;
        *(LDS f32x4*)(tb + 4 * (8 * col + 4 * h)) = f32x4{kacc[0], kacc[1], kacc[2], kacc[3]};
        if (h == 0) {
            *(LDS f32x4*)(tb + 4 * (256 + 4 * col)) = f32x4{kacc[4], kacc[5], kacc[6], kacc[7]};
            *(LDS f32x2*)(tb + 4 * (384 + 2 * col)) = f32x2{kacc[8], kacc[12]};
            *(LDS f32x2*)(tb + 4 * (448 + 2 * col)) = f32x2{kacc[9], kacc[13]};
            *(LDS float*)(tb + 4 * (544 + col)) = kacc[11];
        } else {
            *(LDS float*)(tb + 4 * (576 + col)) = kacc[8];
            *(LDS float*)(tb + 4 * (608 + col)) = kacc[9];
        }
        *(LDS float*)(tb + 4 * (h ? 640 + col : 512 + col)) = kacc[10];
    }
    asm volatile("" :: "v"(pf0));
    post(TRDY(chw));
    await<3, 2, 1>(TRDY(chw));
    {
        const u32 tb = lds0 + SCR + chw * SCRB;
        const int T3 = TAB3 + chw * 2688 - (SCR + chw * SCRB);
        const u32 kb = lds0 + chw * CHB + 56;
        constexpr int T2 = 4 * SCRB;
#define TRD(T, a) ((*(const LDS T*)(a) + *(const LDS T*)((a) + T2) + *(const LDS T*)((a) + T3)) * dt)
        if (part == 0) {
            if (lane < 28) *(LDS u32*)(lds0 + chw * CHB + (lane < 14 ? KC0 + 4 * lane : KC1 + 4 * (lane - 14))) = 0u;
            const bool s1 = lane >= 32;
            const u32 b0 = tb + 4 * (s1 ? 128 + 4 * lane : 8 * lane);
            f32x4 lo = TRD(f32x4, b0), hi = TRD(f32x4, b0 + 16);
            float pm = TRD(float, b0 - (lane == 0 ? 0 : 4));
            if (lane == 0) { lo.x += dsk; pm = 0.f; }
            float e[9];
            e[0] = lane > 32 ? lo.x : pm;
            e[1] = lo.x; e[2] = s1 ? lo.x : lo.y; e[3] = s1 ? lo.y : lo.z; e[4] = s1 ? lo.z : lo.w;
            e[5] = s1 ? lo.z : hi.x; e[6] = s1 ? lo.z : hi.y; e[7] = s1 ? lo.w : hi.z; e[8] = s1 ? hi.x : hi.w;
            *(LDS u32x4*)(kb + KC1 + 16 * lane) = u32x4{pk(e[1], e[2]), pk(e[3], e[4]), pk(e[5], e[6]), pk(e[7], e[8])};
            *(LDS u32x4*)(kb + KC0 + 16 * lane) = u32x4{pk(e[0], e[1]), pk(e[2], e[3]), pk(e[4], e[5]), pk(e[6], e[7])};
        } else {
            if (part == 1) {
                f32x2 v01 = TRD(f32x2, tb + 4 * (384 + 2 * lane));
                float v2 = TRD(float, tb + 4 * (386 + 2 * lane));
                u32 aa = pk(v01.x, v01.x), ab = pk(v01.x, v01.y), bb = pk(v01.y, v01.y), bc = pk(v01.y, v2), cc = pk(v2, v2);
                *(LDS u32x4*)(kb + KC1 + 16 * (64 + lane)) = u32x4{aa, ab, bb, cc};
                *(LDS u32x4*)(kb + KC0 + 16 * (64 + lane)) = u32x4{aa, aa, bb, bc};
            }
            {
                const int it = part - 1;
                float va = TRD(float, tb + 4 * (512 + 64 * it + lane)), vb = TRD(float, tb + 4 * (513 + 64 * it + lane));
                float vm = (lane & 1) ? vb : va;
                u32 aa = pk(va, va), bb = pk(vb, vb);
                *(LDS u32x4*)(kb + KC1 + 16 * (128 + 64 * it + lane)) = u32x4{aa, aa, pk(vm, vb), bb};
                *(LDS u32x4*)(kb + KC0 + 16 * (128 + 64 * it + lane)) = u32x4{aa, aa, pk(va, vm), bb};
            }
        }
    }
    post(KRDY(chw));
    post(KALL);
    if (part == 2) {
        await<4, 3>(SDONE(2));
        epiB(3);
        return;
    }
    f32x4 acc[8];
#pragma unroll
    for (int t = 0; t < 8; ++t) acc[t] = f32x4{0.f, 0.f, 0.f, 0.f};
    const u32 aA = lds0 + chw * CHB + relA, aB = lds0 + chw * CHB + relB;
    const u32 stg = lds0 + SCR + chw * 4608, prt = lds0 + SCR + 4 * 4608 + chw * 3072 + lane * 16;
    await<7, 0, 1>(KRDY(chw));
    if (part == 0) {
        conv_stage<rect(0, 4, 0, 4), true, 8, 2>(acc, aA, aB);
        await<12, 2>(KALL);
        stage_bf16<0>(acc, stg, lane);
        stage_bf16<2>(acc, stg + 1152, lane);
#pragma unroll
        for (int t = 0; t < 3; ++t) *(LDS f32x4*)(prt + 1024 * t) = acc[4 + t];
        post(SDONE(0));
    } else {
        conv_stage<rect(4, 8, 0, 4), true, 8, 1>(acc, aA, aB);
        conv_stage<rect(0, 4, 4, 8), true, 8, 1>(acc, aA, aB);
        await<4, 2>(SDONE(0));
#pragma unroll
        for (int t = 0; t < 3; ++t) acc[4 + t] += *(const LDS f32x4*)(prt + 1024 * t);
        stage_bf16<4>(acc, stg + 2304, lane);
        stage_bf16<6>(acc, stg + 3456, lane);
        post(SDONE(2));
    }
}

extern "C" void kernel_launch(void* const* d_in, const int* in_sizes, int n_in,
                              void* d_out, int out_size, void* d_ws, size_t ws_size,
                              hipStream_t stream) {
    Params P;
    P.q = (const char*)d_in[0]; P.k = (const char*)d_in[1]; P.v = (const char*)d_in[2];
    P.shiftk = (const char*)d_in[3]; P.ssmk = (const char*)d_in[4]; P.ssmD = (const char*)d_in[5];
    P.u = (const char*)d_in[6]; P.Are = (const char*)d_in[7]; P.Aim = (const char*)d_in[8];
    P.Bre = (const char*)d_in[9]; P.Bim = (const char*)d_in[10]; P.Cre = (const char*)d_in[11];
    P.Cim = (const char*)d_in[12]; P.logdt = (const char*)d_in[13]; P.Dskip = (const char*)d_in[14];
    P.out = (char*)d_out;
    fused_kernel<<<256, 1024, 0, stream>>>(P);
}
```
